```python
import torch

def _softmax(x, axis=-1):
    return torch.softmax(x, dim=axis)
def _gelu(x):
    return torch.nn.functional.gelu(x, approximate="tanh")
def _silu(x):
    return torch.nn.functional.silu(x)
def _sigmoid(x):
    return torch.sigmoid(x)
def _layernorm(x, axis=-1, eps=1e-5):
    m = x.mean(dim=axis, keepdim=True)
    v = x.var(dim=axis, keepdim=True, unbiased=False)
    return (x - m) / torch.sqrt(v + eps)
def _scatter_add(tgt, idx, src, axis=0):
    return tgt.clone().index_add_(axis, idx, src)
def _segment_sum(src, seg, n):
    out = torch.zeros((n,) + src.shape[1:], dtype=src.dtype)
    return out.index_add_(0, seg, src)
def _pad(x, pw):
    flat = tuple(v for pair in reversed(pw) for v in pair)
    return torch.nn.functional.pad(x, flat)

kernel_name = 'standalone__paged_block_table_kv_radixattention__sampling'

def setup_inputs():

    _w = (lambda a: (lambda t: t.to(torch.bfloat16) if t.is_floating_point() else t)(torch.as_tensor(a)))
    B, H, Hkv, Dh = 8, 32, 8, 128
    Lk = 8192
    bs = 16
    nb = max(2, Lk // bs)
    P = max(1, nb // 2)
    n_phys = P + B * (nb - P)
    shared = torch.arange(P, dtype=torch.int64)
    priv = P + torch.arange(B * (nb - P), dtype=torch.int64).reshape(B, nb - P)
    bt = torch.cat([torch.tile(shared[None, :], (B, 1)), priv], dim=1)
    ctx = nb * bs - (torch.arange(1, B + 1, dtype=torch.int64) * 2654435761) % max(1, (nb * bs - P * bs) // 2)
    return {
        "q": _w((lambda *s: torch.randn(*s, dtype=torch.bfloat16) * 0.8)(B, H, Dh)),
        "k_cache": _w((lambda *s: torch.randn(*s, dtype=torch.bfloat16) * 0.8)(n_phys, bs, Hkv, Dh)),
        "v_cache": _w((lambda *s: torch.randn(*s, dtype=torch.bfloat16) * 0.05)(n_phys, bs, Hkv, Dh)),
        "block_table": _w(bt.to(torch.int64)),
        "ctx_lens": _w(torch.as_tensor(ctx).to(torch.int64)),
    }

def reference(q, k_cache, v_cache, block_table, ctx_lens):
    B, H, Dh = q.shape
    n_phys, bs, Hkv, _ = k_cache.shape
    nb = block_table.shape[1]
    Lk = nb * bs
    rep = H // Hkv
    k = k_cache[block_table].reshape(B, Lk, Hkv, Dh)
    v = v_cache[block_table].reshape(B, Lk, Hkv, Dh)
    k = (lambda x, n, axis: torch.repeat_interleave(x, n, dim=axis))(k, rep, axis=2)
    v = (lambda x, n, axis: torch.repeat_interleave(x, n, dim=axis))(v, rep, axis=2)
    scores = torch.einsum("bhd,bkhd->bhk", q, k) * (Dh ** -0.5)
    pos = torch.arange(Lk)
    valid = pos[None, :] < ctx_lens[:, None]
    scores = torch.where(valid[:, None, :], scores, -1e9)
    p = _softmax(scores, axis=-1)
    return torch.einsum("bhk,bkhd->bhd", p, v)
```

```cpp
#include <hip/hip_runtime.h>
#include <hip/hip_bf16.h>
#include <cstdint>

namespace {

constexpr int B = 8, H = 32, HKV = 8, G = H / HKV, D = 128, BS = 16, NB = 512, LK = NB * BS;
constexpr int NWG = 256, NW = 8, NT = NW * 64;
constexpr int ROWS = B * G;
constexpr int PAGE_BYTES = BS * HKV * D * 2;
constexpr int TOK_BYTES = HKV * D * 2;
constexpr float SCL = 0.08838834764831845f * 1.4426950408889634f;
constexpr float MINIT = -1e30f;

constexpr int GUESTS = 2;
constexpr int NSF = 32 - GUESTS, NSS = 32 + 3 * GUESTS;
constexpr int SLOTS = 48;
constexpr int MAX_ITEMS = 24;
constexpr int WARM = 2;
constexpr int CODE_LINES = 215;

#define PACE 65.5f
#define T0_NS 2400
#define EARLY_NS 230.0f
#define GUEST_NS 200.0f
#define SPRINT_NS 170
#define POLL_NS 580
#define FA 0.23f
#define FB 0.36f
#define TP_NS 300
#define TC_NS 1300
#define E0_NS 825.0f
#define GX1 7.0f
#define XB 650.0f
#define GX2 3.0f
#define XMAX 2300.0f
constexpr int LEAD = 4, TOUCH_AT = 3, REF_A = 14, REF_B = 23;
constexpr unsigned SPIN_CAP = 1u << 20;

constexpr unsigned WS_EPOCH = 0;
constexpr unsigned WS_HDR = 4096;
constexpr unsigned WS_PROBE = 8192;
constexpr unsigned WS_PO = WS_PROBE + 128 * 128;
constexpr unsigned WS_END = WS_PO + HKV * ROWS * SLOTS * 1024;

constexpr int AUX_NT = 2, AUX_SC1 = 16;
constexpr unsigned OOB = 0x80000000u;

using u32x4 = __attribute__((ext_vector_type(4))) unsigned;
using u32x2 = __attribute__((ext_vector_type(2))) unsigned;
using f32x4 = __attribute__((ext_vector_type(4))) float;
using f32x2 = __attribute__((ext_vector_type(2))) float;
using bf16x8 = __attribute__((ext_vector_type(8))) __bf16;
using bf16x2 = __attribute__((ext_vector_type(2))) __bf16;
using s16x4 = __attribute__((ext_vector_type(4))) short;
typedef __attribute__((address_space(1))) unsigned gu32;

constexpr int OROW = D + 4, BTW = 68, QROW = 17;
struct Lds {
  union {
    struct {
      u32x4 q[ROWS][QROW];
      int bt[B][NW][BTW];
    } pro;
    float o[NW][ROWS][OROW];
  } u;
  f32x2 ml[NW][ROWS];
  u32x2 items[NW][MAX_ITEMS];
  u32x4 prod[NW];
  u32x2 own[NW];
  unsigned tag;
  unsigned done;
  unsigned ready;
  unsigned lat;
  int ext;
  unsigned claim;
  unsigned pad[2];
};
constexpr int EXT_NONE = -0x40000000;

#define WG_RELAXED __ATOMIC_RELAXED, __HIP_MEMORY_SCOPE_WORKGROUP
#define DEV_RELAXED __ATOMIC_RELAXED, __HIP_MEMORY_SCOPE_AGENT

__device__ __forceinline__ __amdgpu_buffer_rsrc_t make_rsrc(const void* p, unsigned bytes) {
  return __builtin_amdgcn_make_buffer_rsrc((void*)p, 0, bytes, 0x00020000);
}
__device__ __forceinline__ void lds_barrier() { asm volatile("s_waitcnt lgkmcnt(0)\n\ts_barrier" ::: "memory"); }
__device__ __forceinline__ unsigned now_ns() { return (unsigned)__builtin_amdgcn_s_memrealtime() * 10u; }
__device__ __forceinline__ bool loads_pending() { return (__builtin_amdgcn_s_getreg(7 | (3 << 11)) & 15) != 0; }

template <int CTRL>
__device__ __forceinline__ unsigned dpp(unsigned x) {
  return (unsigned)__builtin_amdgcn_mov_dpp((int)x, CTRL, 0xf, 0xf, true);
}
template <int CTRL>
__device__ __forceinline__ float dppf(float x) {
  return __uint_as_float(dpp<CTRL>(__float_as_uint(x)));
}
constexpr int XOR1 = 0xB1, XOR2 = 0x4E, HMIR = 0x141, ROR8 = 0x128;

__device__ __forceinline__ float xrow_max(float x) {
  auto s = __builtin_amdgcn_permlane16_swap(__float_as_uint(x), __float_as_uint(x), false, false);
  x = fmaxf(__uint_as_float(s[0]), __uint_as_float(s[1]));
  auto t = __builtin_amdgcn_permlane32_swap(__float_as_uint(x), __float_as_uint(x), false, false);
  return fmaxf(__uint_as_float(t[0]), __uint_as_float(t[1]));
}
__device__ __forceinline__ float xrow_sum(float x) {
  auto s = __builtin_amdgcn_permlane16_swap(__float_as_uint(x), __float_as_uint(x), false, false);
  x = __uint_as_float(s[0]) + __uint_as_float(s[1]);
  auto t = __builtin_amdgcn_permlane32_swap(__float_as_uint(x), __float_as_uint(x), false, false);
  return __uint_as_float(t[0]) + __uint_as_float(t[1]);
}
__device__ __forceinline__ float max8(float x) {
  x = fmaxf(x, dppf<XOR1>(x));
  x = fmaxf(x, dppf<XOR2>(x));
  return fmaxf(x, dppf<HMIR>(x));
}
__device__ __forceinline__ float sum8(float x) {
  x += dppf<XOR1>(x);
  x += dppf<XOR2>(x);
  return x + dppf<HMIR>(x);
}
__device__ __forceinline__ float sum16(float x) {
  x = sum8(x);
  return x + dppf<ROR8>(x);
}
__device__ __forceinline__ unsigned or8(unsigned x) {
  x |= dpp<XOR1>(x);
  x |= dpp<XOR2>(x);
  return x | dpp<HMIR>(x);
}
__device__ __forceinline__ unsigned wave_or(unsigned x) {
  x = or8(x);
  x |= dpp<ROR8>(x);
  auto s = __builtin_amdgcn_permlane16_swap(x, x, false, false);
  x = s[0] | s[1];
  auto t = __builtin_amdgcn_permlane32_swap(x, x, false, false);
  return t[0] | t[1];
}
__device__ __forceinline__ unsigned long long or8_u64(unsigned lo, unsigned hi) {
  return (unsigned)__builtin_amdgcn_readfirstlane(or8(lo)) | ((unsigned long long)(unsigned)__builtin_amdgcn_readfirstlane(or8(hi)) << 32);
}
__device__ __forceinline__ unsigned pack_bf16(float lo, float hi) {
  return __builtin_bit_cast(unsigned, __builtin_convertvector(f32x2{lo, hi}, bf16x2));
}
__device__ __forceinline__ int mbcnt64(unsigned long long m, unsigned z = 0u) {
  return (int)__builtin_amdgcn_mbcnt_hi((unsigned)(m >> 32), __builtin_amdgcn_mbcnt_lo((unsigned)m, z));
}

#define XCHG_BODY(QP)                                                                 \
  "v_cndmask_b32_dpp %0, %12, %8, vcc " QP " row_mask:0xf bank_mask:0xf\n\t"          \
  "v_cndmask_b32_dpp %1, %13, %9, vcc " QP " row_mask:0xf bank_mask:0xf\n\t"          \
  "v_cndmask_b32_dpp %2, %14, %10, vcc " QP " row_mask:0xf bank_mask:0xf\n\t"         \
  "v_cndmask_b32_dpp %3, %15, %11, vcc " QP " row_mask:0xf bank_mask:0xf\n\t"         \
  "s_not_b64 vcc, vcc\n\t"                                                            \
  "v_cndmask_b32_dpp %4, %8, %12, vcc " QP " row_mask:0xf bank_mask:0xf\n\t"          \
  "v_cndmask_b32_dpp %5, %9, %13, vcc " QP " row_mask:0xf bank_mask:0xf\n\t"          \
  "v_cndmask_b32_dpp %6, %10, %14, vcc " QP " row_mask:0xf bank_mask:0xf\n\t"         \
  "v_cndmask_b32_dpp %7, %11, %15, vcc " QP " row_mask:0xf bank_mask:0xf"
#define XCHG_OPS                                                                              \
  : "=&v"(x0), "=&v"(x1), "=&v"(x2), "=&v"(x3), "=&v"(y0), "=&v"(y1), "=&v"(y2), "=&v"(y3)    \
  : "v"(a[0]), "v"(a[1]), "v"(a[2]), "v"(a[3]), "v"(b[0]), "v"(b[1]), "v"(b[2]), "v"(b[3])    \
  : "vcc", "scc"
template <int BIT>
__device__ __forceinline__ void xchg(const u32x4& a, const u32x4& b, u32x4& x, u32x4& y) {
  unsigned x0, x1, x2, x3, y0, y1, y2, y3;
  if constexpr (BIT == 0)
    asm volatile("s_mov_b32 vcc_lo, 0x55555555\n\ts_mov_b32 vcc_hi, 0x55555555\n\t" XCHG_BODY("quad_perm:[1,0,3,2]") XCHG_OPS);
  else
    asm volatile("s_mov_b32 vcc_lo, 0x33333333\n\ts_mov_b32 vcc_hi, 0x33333333\n\t" XCHG_BODY("quad_perm:[2,3,0,1]") "\n\ts_nop 1" XCHG_OPS);
  x = u32x4{x0, x1, x2, x3};
  y = u32x4{y0, y1, y2, y3};
}

__global__ void __launch_bounds__(NT, 2)
attn_paged(const __hip_bfloat16* __restrict__ q, const char* __restrict__ kc, const char* __restrict__ vc,
           const int* __restrict__ bt, const int* __restrict__ ctx, __hip_bfloat16* __restrict__ out, char* ws,
           unsigned cache_bytes) {
  __shared__ __attribute__((aligned(16))) Lds lds;
  const int tid = threadIdx.x, lane = tid & 63;
  const int wave = __builtin_amdgcn_readfirstlane(tid >> 6);
  const int n = lane & 15, g = lane >> 4;
  const int bid = blockIdx.x;
  const int hx = bid & 7, ix = bid >> 3;
  const int col = NW * lane + wave;

  const unsigned tstart = (unsigned)__builtin_amdgcn_s_memrealtime();
  unsigned cpf;
  {
    unsigned long long pc;
    asm volatile("s_getpc_b64 %0" : "=s"(pc));
    cpf = *(const gu32*)((pc & ~63ull) + (tid < CODE_LINES ? tid : 0) * 64u);
  }
  unsigned bto = (unsigned)(wave * NB * 4 + lane * 16);
  unsigned wvo = (unsigned)((lane >> 5) * PAGE_BYTES + ((lane & 31) >> 1) * TOK_BYTES + (lane & 1) * 128);
  unsigned wso = (unsigned)(ix * 3 * WARM) * (unsigned)PAGE_BYTES + (unsigned)(hx * D * 2);
  asm volatile("" : "+v"(bto), "+v"(wvo), "+s"(wso));
  asm volatile("" ::"s"(cache_bytes), "s"(kc), "s"(out));
  __builtin_amdgcn_sched_barrier(0);

  const u32x4 bt0 = *reinterpret_cast<const u32x4*>(reinterpret_cast<const char*>(bt) + bto);
  const u32x4 bt1 = *reinterpret_cast<const u32x4*>(reinterpret_cast<const char*>(bt) + bto + 1024);
  int cl[B];
#pragma unroll
  for (int b = 0; b < B; ++b) cl[b] = ctx[b];
  __builtin_amdgcn_sched_barrier(0);
  asm volatile("s_barrier" ::: "memory");

  unsigned pf[6];
  auto warm = [&](int slot) {
    const unsigned nrec = wave == NW - 1 ? cache_bytes : 0u, so = wso + (unsigned)(slot * WARM * PAGE_BYTES);
    pf[2 * slot] = __builtin_amdgcn_raw_buffer_load_b32(make_rsrc(kc, nrec), wvo, so, 0);
    pf[2 * slot + 1] = __builtin_amdgcn_raw_buffer_load_b32(make_rsrc(vc, nrec), wvo, so, 0);
    __builtin_amdgcn_sched_barrier(0);
  };
  static_assert(WARM == 2, "one load instruction covers two pages");
  warm(0);
  const int hs = (hx & 4) | ((1 - (int)((size_t)kc >> 8)) & 3);
  const bool guest = hx != hs && ix >= NSF;
  int h = guest ? hs : hx;
  const int spg = ((hx - hs - 1) & 3) * GUESTS - NSF, spr = hx == hs ? 3 * GUESTS : 0;
  int sp = ix + (guest ? spg : spr);
  asm volatile("" : "+s"(h), "+s"(sp));
  u32x4 qv = reinterpret_cast<const u32x4*>(q + (size_t)(wave * H + h * G) * D)[lane];
  const unsigned epoch = __hip_atomic_load((gu32*)(ws + WS_EPOCH), DEV_RELAXED);
  __builtin_amdgcn_sched_barrier(0);
  asm volatile("" ::"s"(cl[0]));
  __builtin_amdgcn_sched_barrier(0);
  warm(1);
  asm volatile("" : "+s"(cl[0]), "+s"(cl[1]), "+s"(cl[2]), "+s"(cl[3]), "+s"(cl[4]), "+s"(cl[5]), "+s"(cl[6]), "+s"(cl[7]));

  int cw = cl[0];
#pragma unroll
  for (int b = 1; b < B; ++b) cw = (wave == b) ? cl[b] : cw;
  unsigned long long live[B];
  unsigned lvb = 0;
#pragma unroll
  for (int b = 0; b < B; ++b) {
    cl[b] = (cl[b] <= 0 || cl[b] > LK) ? LK : cl[b];
    live[b] = __builtin_amdgcn_ballot_w64(col * BS < cl[b]);
    lvb |= (col * BS < cl[b]) ? (1u << b) : 0u;
  }
  int ctl[2];
#pragma unroll
  for (int hf = 0; hf < 2; ++hf) {
    int c = cl[4 * hf];
#pragma unroll
    for (int a = 1; a < 4; ++a) c = ((n >> 2) == a) ? cl[4 * hf + a] : c;
    ctl[hf] = c - 4 * g;
  }
  asm volatile("" : "+v"(lvb), "+v"(ctl[0]), "+v"(ctl[1]));
#pragma unroll
  for (int e = 0; e < 4; ++e) {
    lds.u.pro.bt[wave][4 * (lane & 1) + e][lane >> 1] = (int)bt0[e];
    lds.u.pro.bt[wave][4 * (lane & 1) + e][32 + (lane >> 1)] = (int)bt1[e];
  }
  lds_barrier();
  warm(2);

  int pv[B];
#pragma unroll
  for (int b = 0; b < B; ++b) pv[b] = lds.u.pro.bt[b][wave][lane];
  unsigned long long lead[B], heavy[B];
  unsigned eqb = 0;
  {
    unsigned long long eq[B];
#pragma unroll
    for (int b = 0; b < B; ++b) {
      pv[b] = (col * BS < cl[b]) ? pv[b] : (b == 0 ? (int)0x80000000 : pv[b - 1]);
      eq[b] = b == 0 ? 0ull : __builtin_amdgcn_ballot_w64(pv[b] == pv[b - 1]);
      if (b) eqb |= (pv[b] == pv[b - 1]) ? (1u << b) : 0u;
    }
    unsigned long long fol = 0;
#pragma unroll
    for (int b = B - 1; b >= 0; --b) {
      lead[b] = live[b] & ~eq[b];
      heavy[b] = lead[b] & fol;
      fol = eq[b] & (live[b] | fol);
    }
  }
  asm volatile("" : "+v"(eqb));
  auto members = [&](int b) { return (((2u << __builtin_ctz(~(eqb >> (b + 1)))) - 1u) << b) & lvb; };
  int base[B + 1], cnt = 0;
  base[0] = 0;
#pragma unroll
  for (int b = 0; b < B; ++b) {
    const int c = __builtin_popcountll(lead[b]);
    cnt += c;
    base[b + 1] = base[b] + 8 * c + __builtin_popcountll(heavy[b]);
  }
  const int total = base[B];
  const unsigned ta = (unsigned)(total * sp);
  const unsigned mg = h == hs ? 0xFFFFFFFFu / NSS + 1u : 0xFFFFFFFFu / NSF + 1u;
  const int lo = (int)__umulhi(ta, mg) + wave, hi = (int)__umulhi(ta + (unsigned)total, mg) + wave;
  int nmine = 0;
#pragma unroll
  for (int b = 0; b < B; ++b) {
    if (base[b] + 7 < hi && base[b + 1] > lo) {
      const int pos = base[b] + 7 - lo + 8 * mbcnt64(lead[b]) + mbcnt64(heavy[b]);
      const unsigned long long sel = lead[b] & __builtin_amdgcn_ballot_w64((unsigned)pos < (unsigned)(hi - lo));
      if (__builtin_amdgcn_inverse_ballot_w64(sel)) lds.items[wave][nmine + mbcnt64(sel)] = u32x2{(unsigned)pv[b], members(b) | ((unsigned)col << 8)};
      nmine += __builtin_popcountll(sel);
    }
  }
  __builtin_amdgcn_fence(__ATOMIC_RELEASE, "wavefront");
  __builtin_amdgcn_wave_barrier();
  __builtin_amdgcn_fence(__ATOMIC_ACQUIRE, "wavefront");
  u32x2 myit = lds.items[wave][lane < MAX_ITEMS ? lane : 0];

  const unsigned koff = (unsigned)(4 * (n >> 2) * TOK_BYTES + g * 64 + (n & 3) * 16);
  const unsigned voff = (unsigned)(4 * g * TOK_BYTES + n * 16);
  const unsigned hoff = (unsigned)(h * D * 2);
  u32x4 kr[4], vr[4];
  auto issue = [&](u32x4 (&r)[4], const char* src, unsigned off, int i) {
    const unsigned phys = (unsigned)__builtin_amdgcn_readlane((int)myit[0], i);
    const __amdgpu_buffer_rsrc_t rs = make_rsrc(src, i < nmine ? cache_bytes : 0u);
    const unsigned so = phys * (unsigned)PAGE_BYTES + hoff;
#pragma unroll
    for (int t = 0; t < 4; ++t) r[t] = __builtin_amdgcn_raw_buffer_load_b128(rs, off + TOK_BYTES * t, so, AUX_NT);
  };
  issue(kr, kc, koff, 0);
  __builtin_amdgcn_sched_barrier(0);

  myit[1] = lane < nmine ? myit[1] : 0u;
  {
    const unsigned um = wave_or(myit[1] & 0xFFu);
    if (lane == 0) lds.own[wave] = u32x2{um, (unsigned)cnt};
  }
  if (cw <= 0) qv = u32x4{0u, 0u, 0u, 0u};
  lds.u.pro.q[wave * G + g][n] = qv;
  if (wave == 0 && lane == 0) {
    lds.tag = epoch + 1u;
    lds.done = 0u;
    lds.ready = 0u;
    lds.lat = 0u;
    lds.ext = EXT_NONE;
    lds.claim = 0u;
  }
  lds_barrier();
  u32x4 qf[2][4];
#pragma unroll
  for (int hf = 0; hf < 2; ++hf)
#pragma unroll
    for (int s = 0; s < 4; ++s) qf[hf][s] = lds.u.pro.q[hf * 16 + n][4 * g + s];
  const u32x2 ownv = lds.own[lane & 7];
  const unsigned uall = __builtin_amdgcn_readfirstlane(or8(ownv[0]));
  const int nseq = __builtin_popcount(uall);
  const unsigned tag = __builtin_amdgcn_readfirstlane(lds.tag), tg8 = tag << 8;
  const __amdgpu_buffer_rsrc_t rws = make_rsrc(ws, WS_END);
  const unsigned rbase = (unsigned)((hx * ROWS + ix) * SLOTS);

  const float span = 0.125f * PACE * sum8((float)ownv[1]);
  const float dur = fmaxf(span - EARLY_NS * (float)(nseq - 1) - (guest ? GUEST_NS : 0.f), 0.f);
  int del = __builtin_amdgcn_readfirstlane((int)(dur * __builtin_amdgcn_rcpf((float)(2 * nmine + (nmine == 0))))) & -(nmine > 0);
  const unsigned tbase = tstart * 10u + T0_NS;
  unsigned due = tbase + (unsigned)(((wave - LEAD) * del) >> 3);
  unsigned tpoll = tbase + POLL_NS + (unsigned)__builtin_amdgcn_readfirstlane((int)span);
  const unsigned tma = tbase + (unsigned)__builtin_amdgcn_readfirstlane((int)(span * FA));
  const unsigned tmb = tbase + (unsigned)__builtin_amdgcn_readfirstlane((int)(span * FB));
  const bool refwg = ix == REF_A || ix == REF_B;
  const unsigned posto = WS_PROBE + (unsigned)(hx + (ix == REF_B ? 8 : 0)) * 128u;
  auto claim = [&](unsigned bit) {
    unsigned old = 0;
    if (lane == 0) old = __hip_atomic_fetch_or(&lds.claim, bit, WG_RELAXED);
    return (__builtin_amdgcn_readfirstlane(old) & bit) == 0u;
  };
  bool settled = false;
  unsigned tlast = 0, topen = 0;
  auto pace = [&](int left) {
    unsigned now = now_ns();
    if (!settled && (int)(now - tma) >= 0) {
      if (topen) {
        if (lane == 0) __hip_atomic_fetch_add(&lds.lat, min(now - topen - 120u, 2500u) + (1u << 20), WG_RELAXED);
        topen = 0;
      }
      if ((int)(now - tmb) < 0) {
        if (refwg) {
          bool pend;
          for (;;) {
            now = now_ns();
            pend = loads_pending();
            if (!pend || (int)(now - (due + del)) >= 0) break;
            __builtin_amdgcn_s_sleep(1);
          }
          if (pend) topen = tlast | 1u;
          else if (lane == 0) __hip_atomic_fetch_add(&lds.lat, min(now - tlast, 2500u) + (1u << 20), WG_RELAXED);
        }
      } else {
        if (refwg && (int)(now - (tmb + TP_NS)) >= 0 && claim(1u)) {
          const unsigned acc = __hip_atomic_load(&lds.lat, WG_RELAXED);
          __builtin_amdgcn_raw_buffer_store_b128(u32x4{acc, tag, acc, tag}, rws, (lane >> 3) * 2048 + (lane & 7) * 16, posto, AUX_SC1);
        } else if ((int)(now - (tmb + TC_NS)) >= 0 && claim(2u)) {
          const u32x2 v = __builtin_amdgcn_raw_buffer_load_b64(rws, lane < 16 ? lane * 128u : OOB, WS_PROBE + (unsigned)hx * 2048u, AUX_SC1);
          const bool ok = lane < 16 && v[1] == tag;
          const float sum = sum16(ok ? (float)(v[0] & 0xFFFFFu) : 0.f), cn = sum16(ok ? (float)(v[0] >> 20) : 0.f);
          const float e = sum * __builtin_amdgcn_rcpf(cn) - E0_NS;
          const float x = fminf(fmaxf(fminf(e * GX1, XB + e * GX2), 0.f), XMAX) * (span * (1.f / 15200.f));
          if (lane == 0) __hip_atomic_store(&lds.ext, cn > 0.f ? (int)x : 0, WG_RELAXED);
        }
        const int x = __builtin_amdgcn_readfirstlane(__hip_atomic_load(&lds.ext, WG_RELAXED));
        if (x != EXT_NONE) {
          settled = true;
          tpoll += (unsigned)x;
          del += __builtin_amdgcn_readfirstlane((int)((float)x * __builtin_amdgcn_rcpf((float)left)));
        }
      }
    }
    due += (unsigned)(left <= 2 ? del - SPRINT_NS : del);
    while ((int)((now = now_ns()) - due) < 0) __builtin_amdgcn_s_sleep(1);
    tlast = now;
  };

  auto producers = [&]() {
    if (wave == 0 && lane == 0) __hip_atomic_store((gu32*)(ws + WS_HDR) + bid, tag, DEV_RELAXED);
    int tb = ix >> 2, idx = 0;
    unsigned z = 0;
    asm volatile("" : "+s"(tb), "+v"(z));
#pragma unroll
    for (int a = 0; a < B; ++a) {
      const int ia = base[a] + 7 + 8 * mbcnt64(lead[a], z) + mbcnt64(heavy[a], z);
      idx = __builtin_amdgcn_inverse_ballot_w64(a <= tb ? lead[a] : 0ull) ? ia : idx;
    }
    const int s2 = (int)(((float)((hx == hs ? NSS : NSF) * (idx - wave + 1) - 1) + 0.5f) * __builtin_amdgcn_rcpf((float)total));
    const unsigned bit = ((lvb >> tb) & 1u) << (s2 & 31), blo = s2 < 32 ? bit : 0u, bhi = s2 < 32 ? 0u : bit;
    const bool shr = ((eqb >> tb) & 1u) || members(tb) != (1u << tb);
    const u32x4 r4 = {wave_or(blo), wave_or(bhi), wave_or(shr ? blo : 0u), wave_or(shr ? bhi : 0u)};
    if (lane == 0) {
      lds.prod[wave] = r4;
      asm volatile("s_waitcnt lgkmcnt(0)" ::: "memory");
      __hip_atomic_fetch_add(&lds.ready, 1u, WG_RELAXED);
    }
  };
  unsigned long long need = 0;
  int nsrc = 0, srcv = 0, rowv = 0;
  unsigned go[2] = {0u, 0u};
  auto slot_off = [&](int k) { return (rbase + (unsigned)__builtin_amdgcn_ds_bpermute(4 * k, srcv)) * 1024u + (lane & 7) * 16u; };
  auto resolve = [&]() {
    while (__builtin_amdgcn_readfirstlane(__hip_atomic_load(&lds.ready, WG_RELAXED)) < NW) __builtin_amdgcn_s_sleep(1);
    asm volatile("" ::: "memory");
    const u32x4 pr = lds.prod[lane & 7];
    need = or8_u64(pr[0], pr[1]);
    const unsigned long long early = or8_u64(pr[2], pr[3]);
    const int seqv = __builtin_amdgcn_ds_permute(lane < B && ((uall >> lane) & 1u) ? 4 * __builtin_popcount(uall & ((1u << (lane & 7)) - 1u)) : 252, lane);
    rowv = 4 * __builtin_amdgcn_ds_bpermute(4 * (lane >> 3), seqv) + (wave & 3);
    nsrc = __builtin_popcountll(need);
    srcv = __builtin_amdgcn_ds_permute(__builtin_amdgcn_inverse_ballot_w64(need) ? 4 * mbcnt64(need) : 252, lane);
#pragma unroll
    for (int t = 0; t < 2; ++t) go[t] = slot_off(8 * t + (lane >> 3));
    return __builtin_amdgcn_inverse_ballot_w64(need & ~early) ? WS_PO + (rbase + (unsigned)lane) * 1024u + wave * 128u : WS_END;
  };
  const int ires = min(TOUCH_AT, nmine - 1);

  f32x4 o[2][8];
  float m[2], l[2];
#pragma unroll
  for (int hf = 0; hf < 2; ++hf) {
    m[hf] = MINIT;
    l[hf] = 0.f;
#pragma unroll
    for (int j = 0; j < 8; ++j) o[hf][j] = f32x4{0.f, 0.f, 0.f, 0.f};
  }
  unsigned tch = 0;
  int i = 0;
  asm volatile("" : "+s"(i));
  do {
    pace(2 * (nmine - i) - 1);
    issue(vr, vc, voff, i);
    __builtin_amdgcn_sched_barrier(0);
    if (i == 0) producers();
    const unsigned meta = (unsigned)__builtin_amdgcn_readlane((int)myit[1], i);
    const int tok0 = (int)(meta >> 8) * BS;
    u32x4 t0, t1, t2, t3, kt[4];
    asm volatile("" ::"v"(tch));
    xchg<0>(kr[0], kr[1], t0, t1);
    xchg<0>(kr[2], kr[3], t2, t3);
    xchg<1>(t0, t2, kt[0], kt[2]);
    xchg<1>(t1, t3, kt[1], kt[3]);
    __builtin_amdgcn_sched_barrier(0);
    unsigned tcho = (refwg && wave == NW - 1 && i == 0 && lane < 8) ? posto + lane * 2048u : WS_END;
    if (i >= ires && nsrc == 0) tcho = resolve();
    if (i + 1 < nmine) pace(2 * (nmine - i) - 2);
    issue(kr, kc, koff, i + 1);
    tch = __builtin_amdgcn_raw_buffer_load_b32(rws, tcho, 0, AUX_SC1);
    __builtin_amdgcn_sched_barrier(0);
    u32x2 pp[2];
#pragma unroll
    for (int hf = 0; hf < 2; ++hf) {
      if ((meta >> (4 * hf)) & 0xFu) {
        f32x4 s = {0.f, 0.f, 0.f, 0.f};
#pragma unroll
        for (int st = 0; st < 4; ++st)
          s = __builtin_amdgcn_mfma_f32_16x16x32_bf16(__builtin_bit_cast(bf16x8, kt[st]), __builtin_bit_cast(bf16x8, qf[hf][st]), s, 0, 0, 0);
        const int lim = ((meta >> (4 * hf + (n >> 2))) & 1u) ? ctl[hf] - tok0 : 0;
        float x[4], e[4];
#pragma unroll
        for (int r = 0; r < 4; ++r) x[r] = (r < lim) ? s[r] * SCL : -INFINITY;
        const float tm = xrow_max(fmaxf(fmaxf(x[0], x[1]), fmaxf(x[2], x[3])));
        const float mn = fmaxf(m[hf], tm);
        const float al = __builtin_amdgcn_exp2f(m[hf] - mn);
#pragma unroll
        for (int r = 0; r < 4; ++r) e[r] = __builtin_amdgcn_exp2f(x[r] - mn);
        l[hf] = l[hf] * al + ((e[0] + e[1]) + (e[2] + e[3]));
        m[hf] = mn;
#pragma unroll
        for (int j = 0; j < 8; ++j) o[hf][j] *= al;
        pp[hf] = u32x2{pack_bf16(e[0], e[1]), pack_bf16(e[2], e[3])};
      }
    }
    if (i + 1 >= nmine) {
#pragma unroll
      for (int hf = 0; hf < 2; ++hf) {
        const float lt = xrow_sum(l[hf]);
        if (g == 0) lds.ml[wave][hf * 16 + n] = f32x2{m[hf], lt};
      }
      asm volatile("s_waitcnt lgkmcnt(0)" ::: "memory");
      if (lane == 0) __hip_atomic_fetch_add(&lds.done, 1u, WG_RELAXED);
    }
    __builtin_amdgcn_sched_barrier(0);
    u32x2 vt[8];
#pragma unroll
    for (int w = 0; w < 4; ++w) {
      vt[2 * w] = u32x2{__builtin_amdgcn_perm(vr[1][w], vr[0][w], 0x05040100u), __builtin_amdgcn_perm(vr[3][w], vr[2][w], 0x05040100u)};
      vt[2 * w + 1] = u32x2{__builtin_amdgcn_perm(vr[1][w], vr[0][w], 0x07060302u), __builtin_amdgcn_perm(vr[3][w], vr[2][w], 0x07060302u)};
    }
#pragma unroll
    for (int hf = 0; hf < 2; ++hf) {
      if ((meta >> (4 * hf)) & 0xFu) {
#pragma unroll
        for (int j = 0; j < 8; ++j)
          o[hf][j] = __builtin_amdgcn_mfma_f32_16x16x16bf16_1k(__builtin_bit_cast(s16x4, vt[j]), __builtin_bit_cast(s16x4, pp[hf]), o[hf][j], 0, 0, 0);
      }
    }
    __builtin_amdgcn_sched_barrier(0);
  } while (++i < nmine);

#pragma unroll
  for (int hf = 0; hf < 2; ++hf) {
    const int row = hf * 16 + n;
    if ((uall >> (row >> 2)) & 1u) {
#pragma unroll
      for (int j = 0; j < 8; ++j) *reinterpret_cast<f32x4*>(&lds.u.o[wave][row][16 * j + 4 * g]) = o[hf][j];
    }
  }
  __builtin_amdgcn_s_waitcnt(0x0F70);
  asm volatile("" ::"v"(cpf), "v"(pf[0]), "v"(pf[1]), "v"(pf[2]), "v"(pf[3]), "v"(pf[4]), "v"(pf[5]));

  const int kstep = nseq > 2 ? 2 : 1, kfirst = wave >> 2;
  if (kfirst < kstep) {
    while (__builtin_amdgcn_readfirstlane(__hip_atomic_load(&lds.done, WG_RELAXED)) < NW) __builtin_amdgcn_s_sleep(1);
    asm volatile("" ::: "memory");
    const f32x2 ml = lds.ml[lane & 7][rowv & 31];
    const float M = max8(ml[0]);
    const float wt = __builtin_amdgcn_exp2f(ml[0] - M);
    const float L = sum8(wt * ml[1]);
    const int wn = (int)__float_as_uint(wt * __builtin_amdgcn_rcpf(L));
    const int lsev = (int)__float_as_uint(M + __builtin_amdgcn_logf(L));
    for (int k = kfirst;; k += kstep) {
      const int row = __builtin_amdgcn_readlane(rowv, 8 * k) & 31;
      f32x2 wp[4];
#pragma unroll
      for (int w = 0; w < NW; ++w) wp[w >> 1][w & 1] = __uint_as_float((unsigned)__builtin_amdgcn_readlane(wn, 8 * k + w));
      const unsigned lh = (unsigned)__builtin_amdgcn_readlane(lsev, 8 * k) >> (16 * (lane & 1));
      unsigned tw0 = tg8 | (lh & 0xFFu), tw1 = tg8 | ((lh >> 8) & 0xFFu);
      int e0 = (row * OROW + 2 * lane) * 4, e1 = e0 + 16 * ROWS * OROW;
      unsigned pso = WS_PO + (unsigned)((h * ROWS + row) * SLOTS + sp) * 1024u;
      asm volatile("" : "+v"(tw0), "+v"(tw1), "+v"(e0), "+v"(e1), "+s"(pso), "+v"(wp[0]), "+v"(wp[1]), "+v"(wp[2]), "+v"(wp[3]));
      if (k == kfirst) lds_barrier();
      if (k >= nseq) break;
      const char* img = reinterpret_cast<const char*>(&lds.u.o[0][0][0]);
      f32x2 ov[NW];
#pragma unroll
      for (int w = 0; w < NW; ++w) ov[w] = *reinterpret_cast<const f32x2*>(img + (w < 4 ? e0 : e1) + (w & 3) * (ROWS * OROW * 4));
      f32x2 a0 = wp[0][0] * ov[0], a1 = wp[2][0] * ov[4];
#pragma unroll
      for (int w = 1; w < 4; ++w) {
        a0 += wp[w >> 1][w & 1] * ov[w];
        a1 += wp[2 + (w >> 1)][w & 1] * ov[4 + w];
      }
      a0 += a1;
      __builtin_amdgcn_raw_buffer_store_b128(u32x4{__float_as_uint(a0[0]), tw0, __float_as_uint(a0[1]), tw1}, rws, lane * 16, pso, AUX_SC1);
      if (k + kstep >= nseq) break;
    }
  } else {
    lds_barrier();
  }
  __builtin_amdgcn_sched_barrier(0);

  if (bid == 0 && wave == NW - 2) {
    for (unsigned spins = 0;; ++spins) {
      const u32x4 hd = __builtin_amdgcn_raw_buffer_load_b128(rws, lane * 16, WS_HDR, AUX_SC1);
      const bool ok = hd[0] == tag && hd[1] == tag && hd[2] == tag && hd[3] == tag;
      if (__builtin_amdgcn_ballot_w64(!ok) == 0ull || spins > SPIN_CAP) break;
      __builtin_amdgcn_s_sleep(2);
      asm volatile("" ::: "memory");
    }
    if (lane == 0) __hip_atomic_store((gu32*)(ws + WS_EPOCH), tag, DEV_RELAXED);
  }

  u32x4 og[2];
  auto gather_init = [&](int k0) {
#pragma unroll
    for (int t = 0; t < 2; ++t) {
      const unsigned tw = (k0 + 8 * t + (lane >> 3) < nsrc) ? ~tg8 : tg8;
      og[t] = u32x4{0u, tw | ((lane & 1) ? 0x80u : 0u), 0u, tw | ((lane & 1) ? 0xFFu : 0u)};
    }
  };
  gather_init(0);
  __builtin_amdgcn_sched_barrier(0);
  if (!guest && ((need >> sp) & 1u)) __builtin_amdgcn_s_sleep(10);
  while ((int)(now_ns() - tpoll) < 0) __builtin_amdgcn_s_sleep(1);
#pragma unroll
  for (int t = 0; t < 2; ++t)
    if (8 * t + (lane >> 3) < nsrc) og[t] = __builtin_amdgcn_raw_buffer_load_b128(rws, go[t], WS_PO + wave * 128u, AUX_SC1);
  float Mr = -INFINITY, Wr = 0.f;
  f32x2 ar = {0.f, 0.f};
  for (int k0 = 0;;) {
    for (unsigned spins = 0;; ++spins) {
      const unsigned b0 = (og[0][1] ^ tg8) | (og[0][3] ^ tg8), b1 = (og[1][1] ^ tg8) | (og[1][3] ^ tg8);
      if (__builtin_amdgcn_ballot_w64((b0 | b1) > 0xFFu) == 0ull || spins >= SPIN_CAP) break;
      __builtin_amdgcn_s_sleep(3);
      asm volatile("" ::: "memory");
      if (b0 > 0xFFu) og[0] = __builtin_amdgcn_raw_buffer_load_b128(rws, go[0], WS_PO + wave * 128u, AUX_SC1);
      if (b1 > 0xFFu) og[1] = __builtin_amdgcn_raw_buffer_load_b128(rws, go[1], WS_PO + wave * 128u, AUX_SC1);
    }
    const unsigned h0 = __builtin_amdgcn_perm(og[0][3], og[0][1], (lane & 1) ? 0x04000c0cu : 0x0c0c0400u);
    const unsigned h1 = __builtin_amdgcn_perm(og[1][3], og[1][1], (lane & 1) ? 0x04000c0cu : 0x0c0c0400u);
    const float l0 = __uint_as_float(h0 | dpp<XOR1>(h0)), l1 = __uint_as_float(h1 | dpp<XOR1>(h1));
    float M = fmaxf(fmaxf(l0, l1), Mr);
    M = xrow_max(fmaxf(M, dppf<ROR8>(M)));
    const float w0 = __builtin_amdgcn_exp2f(l0 - M), w1 = __builtin_amdgcn_exp2f(l1 - M), wr = __builtin_amdgcn_exp2f(Mr - M);
    Wr = Wr * wr + (w0 + w1);
    ar[0] = ar[0] * wr + (w0 * __uint_as_float(og[0][0]) + w1 * __uint_as_float(og[1][0]));
    ar[1] = ar[1] * wr + (w0 * __uint_as_float(og[0][2]) + w1 * __uint_as_float(og[1][2]));
    Mr = M;
    k0 += 16;
    if (k0 >= nsrc) break;
    gather_init(k0);
#pragma unroll
    for (int t = 0; t < 2; ++t) go[t] = slot_off(k0 + 8 * t + (lane >> 3));
  }
  Wr = xrow_sum(Wr + dppf<ROR8>(Wr));
  const auto sw = __builtin_amdgcn_permlane32_swap(__float_as_uint(ar[0]), __float_as_uint(ar[1]), false, false);
  float z = __uint_as_float(sw[0]) + __uint_as_float(sw[1]);
  const auto s16 = __builtin_amdgcn_permlane16_swap(__float_as_uint(z), __float_as_uint(z), false, false);
  z = __uint_as_float(s16[0]) + __uint_as_float(s16[1]);
  z += dppf<ROR8>(z);
  const unsigned pk = pack_bf16(z * __builtin_amdgcn_rcpf(Wr), 0.f);
  const unsigned d = 32u * ((lane >> 1) & 3) + 16u * (lane & 1) + 8u * (lane >> 5) + wave;
  if ((lane & 24) == 0) reinterpret_cast<unsigned short*>(out)[((ix >> 2) * H + hx * G + (ix & 3)) * D + d] = (unsigned short)pk;
}

}

extern "C" void kernel_launch(void* const* d_in, const int* in_sizes, int n_in, void* d_out, int out_size, void* d_ws,
                              size_t ws_size, hipStream_t stream) {
  const unsigned cache_bytes = (unsigned)in_sizes[1] * 2u;
  attn_paged<<<NWG, NT, 0, stream>>>((const __hip_bfloat16*)d_in[0], (const char*)d_in[1], (const char*)d_in[2],
                                     (const int*)d_in[3], (const int*)d_in[4], (__hip_bfloat16*)d_out, (char*)d_ws,
                                     cache_bytes);
}
```

```cpp
#include <hip/hip_runtime.h>
#include <hip/hip_bf16.h>
#include <cstdint>

namespace {

constexpr int B = 8, H = 32, HKV = 8, G = H / HKV, D = 128, BS = 16, NB = 512, LK = NB * BS;
constexpr int NWG = 256, NW = 8, NT = NW * 64;
constexpr int ROWS = B * G;
constexpr int PAGE_BYTES = BS * HKV * D * 2;
constexpr int TOK_BYTES = HKV * D * 2;
constexpr float SCL = 0.08838834764831845f * 1.4426950408889634f;
constexpr float MINIT = -1e30f;

constexpr int GUESTS = 2;
constexpr int NSF = 32 - GUESTS, NSS = 32 + 3 * GUESTS;
constexpr int SLOTS = 48;
constexpr int MAX_ITEMS = 24;
constexpr int WARM = 2;
constexpr int CODE_LINES = 218;

#define PACE 66.5f
#define T0_NS 2400
#define EARLY_NS 290.0f
#define GUEST_NS 300.0f
#define SPRINT_NS 170
#define POLL_NS 650
#define FA 0.23f
#define FB 0.36f
#define ALIGN 1.0f
#define TP_NS 300
#define TC_NS 1300
#define E0_NS 775.0f
#define GX1 5.0f
#define XB 450.0f
#define GX2 2.5f
#define XMIN 0.0f
#define XMAX 2300.0f
constexpr int LEAD = 4, TOUCH_AT = 3, REF_A = 14, REF_B = 23;
constexpr unsigned SPIN_CAP = 1u << 20;

constexpr unsigned WS_EPOCH = 0;
constexpr unsigned WS_HDR = 4096;
constexpr unsigned WS_PROBE = 8192;
constexpr unsigned WS_PO = WS_PROBE + 128 * 128;
constexpr unsigned WS_END = WS_PO + HKV * ROWS * SLOTS * 1024;

constexpr int AUX_NT = 2, AUX_SC1 = 16;
constexpr unsigned OOB = 0x80000000u;

using u32x4 = __attribute__((ext_vector_type(4))) unsigned;
using u32x2 = __attribute__((ext_vector_type(2))) unsigned;
using f32x4 = __attribute__((ext_vector_type(4))) float;
using f32x2 = __attribute__((ext_vector_type(2))) float;
using bf16x8 = __attribute__((ext_vector_type(8))) __bf16;
using bf16x2 = __attribute__((ext_vector_type(2))) __bf16;
using s16x4 = __attribute__((ext_vector_type(4))) short;
typedef __attribute__((address_space(1))) unsigned gu32;

constexpr int OROW = D + 4, BTW = 68, QROW = 17;
struct Lds {
  union {
    struct {
      u32x4 q[ROWS][QROW];
      int bt[B][NW][BTW];
    } pro;
    float o[NW][ROWS][OROW];
  } u;
  f32x2 ml[NW][ROWS];
  u32x2 items[NW][MAX_ITEMS];
  u32x4 prod[NW];
  u32x2 own[NW];
  unsigned tag;
  unsigned done;
  unsigned ready;
  unsigned lat;
  int ext;
  unsigned claim;
  unsigned pad[2];
};
constexpr int EXT_NONE = -0x40000000;

#define WG_RELAXED __ATOMIC_RELAXED, __HIP_MEMORY_SCOPE_WORKGROUP
#define DEV_RELAXED __ATOMIC_RELAXED, __HIP_MEMORY_SCOPE_AGENT

__device__ __forceinline__ __amdgpu_buffer_rsrc_t make_rsrc(const void* p, unsigned bytes) {
  return __builtin_amdgcn_make_buffer_rsrc((void*)p, 0, bytes, 0x00020000);
}
__device__ __forceinline__ void lds_barrier() { asm volatile("s_waitcnt lgkmcnt(0)\n\ts_barrier" ::: "memory"); }
__device__ __forceinline__ unsigned now_ns() { return (unsigned)__builtin_amdgcn_s_memrealtime() * 10u; }
__device__ __forceinline__ bool loads_pending() { return (__builtin_amdgcn_s_getreg(7 | (3 << 11)) & 15) != 0; }

template <int CTRL>
__device__ __forceinline__ unsigned dpp(unsigned x) {
  return (unsigned)__builtin_amdgcn_mov_dpp((int)x, CTRL, 0xf, 0xf, true);
}
template <int CTRL>
__device__ __forceinline__ float dppf(float x) {
  return __uint_as_float(dpp<CTRL>(__float_as_uint(x)));
}
constexpr int XOR1 = 0xB1, XOR2 = 0x4E, HMIR = 0x141, ROR8 = 0x128;

__device__ __forceinline__ float xrow_max(float x) {
  auto s = __builtin_amdgcn_permlane16_swap(__float_as_uint(x), __float_as_uint(x), false, false);
  x = fmaxf(__uint_as_float(s[0]), __uint_as_float(s[1]));
  auto t = __builtin_amdgcn_permlane32_swap(__float_as_uint(x), __float_as_uint(x), false, false);
  return fmaxf(__uint_as_float(t[0]), __uint_as_float(t[1]));
}
__device__ __forceinline__ float xrow_sum(float x) {
  auto s = __builtin_amdgcn_permlane16_swap(__float_as_uint(x), __float_as_uint(x), false, false);
  x = __uint_as_float(s[0]) + __uint_as_float(s[1]);
  auto t = __builtin_amdgcn_permlane32_swap(__float_as_uint(x), __float_as_uint(x), false, false);
  return __uint_as_float(t[0]) + __uint_as_float(t[1]);
}
__device__ __forceinline__ float group_max(float a, float b) {
  float r, t;
  asm volatile(
      "v_max_f32 %0, %2, %3\n\ts_nop 1\n\t"
      "v_max_f32_dpp %0, %0, %0 row_ror:8 row_mask:0xf bank_mask:0xf\n\t"
      "v_mov_b32 %1, %0\n\ts_nop 1\n\t"
      "v_permlane16_swap_b32 %0, %1\n\t"
      "v_max_f32 %0, %0, %1\n\t"
      "v_mov_b32 %1, %0\n\ts_nop 1\n\t"
      "v_permlane32_swap_b32 %0, %1\n\t"
      "v_max_f32 %0, %0, %1"
      : "=&v"(r), "=&v"(t)
      : "v"(a), "v"(b));
  return r;
}
__device__ __forceinline__ float max8(float x) {
  x = fmaxf(x, dppf<XOR1>(x));
  x = fmaxf(x, dppf<XOR2>(x));
  return fmaxf(x, dppf<HMIR>(x));
}
__device__ __forceinline__ float sum8(float x) {
  x += dppf<XOR1>(x);
  x += dppf<XOR2>(x);
  return x + dppf<HMIR>(x);
}
__device__ __forceinline__ float sum16(float x) {
  x = sum8(x);
  return x + dppf<ROR8>(x);
}
__device__ __forceinline__ unsigned or8(unsigned x) {
  x |= dpp<XOR1>(x);
  x |= dpp<XOR2>(x);
  return x | dpp<HMIR>(x);
}
__device__ __forceinline__ unsigned wave_or(unsigned x) {
  x = or8(x);
  x |= dpp<ROR8>(x);
  auto s = __builtin_amdgcn_permlane16_swap(x, x, false, false);
  x = s[0] | s[1];
  auto t = __builtin_amdgcn_permlane32_swap(x, x, false, false);
  return t[0] | t[1];
}
__device__ __forceinline__ unsigned long long or8_u64(unsigned lo, unsigned hi) {
  return (unsigned)__builtin_amdgcn_readfirstlane(or8(lo)) | ((unsigned long long)(unsigned)__builtin_amdgcn_readfirstlane(or8(hi)) << 32);
}
__device__ __forceinline__ unsigned pack_bf16(float lo, float hi) {
  return __builtin_bit_cast(unsigned, __builtin_convertvector(f32x2{lo, hi}, bf16x2));
}
__device__ __forceinline__ int mbcnt64(unsigned long long m, unsigned z = 0u) {
  return (int)__builtin_amdgcn_mbcnt_hi((unsigned)(m >> 32), __builtin_amdgcn_mbcnt_lo((unsigned)m, z));
}

#define XCHG_BODY(QP)                                                                 \
  "v_cndmask_b32_dpp %0, %12, %8, vcc " QP " row_mask:0xf bank_mask:0xf\n\t"          \
  "v_cndmask_b32_dpp %1, %13, %9, vcc " QP " row_mask:0xf bank_mask:0xf\n\t"          \
  "v_cndmask_b32_dpp %2, %14, %10, vcc " QP " row_mask:0xf bank_mask:0xf\n\t"         \
  "v_cndmask_b32_dpp %3, %15, %11, vcc " QP " row_mask:0xf bank_mask:0xf\n\t"         \
  "s_not_b64 vcc, vcc\n\t"                                                            \
  "v_cndmask_b32_dpp %4, %8, %12, vcc " QP " row_mask:0xf bank_mask:0xf\n\t"          \
  "v_cndmask_b32_dpp %5, %9, %13, vcc " QP " row_mask:0xf bank_mask:0xf\n\t"          \
  "v_cndmask_b32_dpp %6, %10, %14, vcc " QP " row_mask:0xf bank_mask:0xf\n\t"         \
  "v_cndmask_b32_dpp %7, %11, %15, vcc " QP " row_mask:0xf bank_mask:0xf"
#define XCHG_OPS                                                                              \
  : "=&v"(x0), "=&v"(x1), "=&v"(x2), "=&v"(x3), "=&v"(y0), "=&v"(y1), "=&v"(y2), "=&v"(y3)    \
  : "v"(a[0]), "v"(a[1]), "v"(a[2]), "v"(a[3]), "v"(b[0]), "v"(b[1]), "v"(b[2]), "v"(b[3])    \
  : "vcc", "scc"
template <int BIT>
__device__ __forceinline__ void xchg(const u32x4& a, const u32x4& b, u32x4& x, u32x4& y) {
  unsigned x0, x1, x2, x3, y0, y1, y2, y3;
  if constexpr (BIT == 0)
    asm volatile("s_mov_b32 vcc_lo, 0x55555555\n\ts_mov_b32 vcc_hi, 0x55555555\n\t" XCHG_BODY("quad_perm:[1,0,3,2]") XCHG_OPS);
  else
    asm volatile("s_mov_b32 vcc_lo, 0x33333333\n\ts_mov_b32 vcc_hi, 0x33333333\n\t" XCHG_BODY("quad_perm:[2,3,0,1]") "\n\ts_nop 1" XCHG_OPS);
  x = u32x4{x0, x1, x2, x3};
  y = u32x4{y0, y1, y2, y3};
}

__global__ void __launch_bounds__(NT, 2)
attn_paged(const __hip_bfloat16* __restrict__ q, const char* __restrict__ kc, const char* __restrict__ vc,
           const int* __restrict__ bt, const int* __restrict__ ctx, __hip_bfloat16* __restrict__ out, char* ws,
           unsigned cache_bytes) {
  __shared__ __attribute__((aligned(16))) Lds lds;
  const int tid = threadIdx.x, lane = tid & 63;
  const int wave = __builtin_amdgcn_readfirstlane(tid >> 6);
  const int n = lane & 15, g = lane >> 4;
  const int bid = blockIdx.x;
  const int hx = bid & 7, ix = bid >> 3;
  const int col = NW * lane + wave;

  const unsigned tstart = (unsigned)__builtin_amdgcn_s_memrealtime();
  unsigned cpf;
  {
    unsigned long long pc;
    asm volatile("s_getpc_b64 %0" : "=s"(pc));
    cpf = *(const gu32*)((pc & ~63ull) + (tid < CODE_LINES ? tid : 0) * 64u);
  }
  unsigned bto = (unsigned)(wave * NB * 4 + lane * 16);
  unsigned wvo = (unsigned)((lane >> 5) * PAGE_BYTES + ((lane & 31) >> 1) * TOK_BYTES + (lane & 1) * 128);
  unsigned wso = (unsigned)(ix * 3 * WARM) * (unsigned)PAGE_BYTES + (unsigned)(hx * D * 2);
  asm volatile("" : "+v"(bto), "+v"(wvo), "+s"(wso));
  asm volatile("" ::"s"(cache_bytes), "s"(kc), "s"(out));
  __builtin_amdgcn_sched_barrier(0);

  const u32x4 bt0 = *reinterpret_cast<const u32x4*>(reinterpret_cast<const char*>(bt) + bto);
  const u32x4 bt1 = *reinterpret_cast<const u32x4*>(reinterpret_cast<const char*>(bt) + bto + 1024);
  int cl[B];
#pragma unroll
  for (int b = 0; b < B; ++b) cl[b] = ctx[b];
  __builtin_amdgcn_sched_barrier(0);
  asm volatile("s_barrier" ::: "memory");

  unsigned pf[6];
  auto warm = [&](int slot) {
    const unsigned nrec = wave == NW - 1 ? cache_bytes : 0u, so = wso + (unsigned)(slot * WARM * PAGE_BYTES);
    pf[2 * slot] = __builtin_amdgcn_raw_buffer_load_b32(make_rsrc(kc, nrec), wvo, so, 0);
    pf[2 * slot + 1] = __builtin_amdgcn_raw_buffer_load_b32(make_rsrc(vc, nrec), wvo, so, 0);
    __builtin_amdgcn_sched_barrier(0);
  };
  static_assert(WARM == 2, "one load instruction covers two pages");
  warm(0);
  const int hs = (hx & 4) | ((1 - (int)((size_t)kc >> 8)) & 3);
  const bool guest = hx != hs && ix >= NSF;
  int h = guest ? hs : hx;
  const int spg = ((hx - hs - 1) & 3) * GUESTS - NSF, spr = hx == hs ? 3 * GUESTS : 0;
  int sp = ix + (guest ? spg : spr);
  asm volatile("" : "+s"(h), "+s"(sp));
  u32x4 qv = reinterpret_cast<const u32x4*>(q + (size_t)(wave * H + h * G) * D)[lane];
  const unsigned epoch = __hip_atomic_load((gu32*)(ws + WS_EPOCH), DEV_RELAXED);
  __builtin_amdgcn_sched_barrier(0);
  asm volatile("" ::"s"(cl[0]));
  __builtin_amdgcn_sched_barrier(0);
  warm(1);
  asm volatile("" : "+s"(cl[0]), "+s"(cl[1]), "+s"(cl[2]), "+s"(cl[3]), "+s"(cl[4]), "+s"(cl[5]), "+s"(cl[6]), "+s"(cl[7]));

  int cw = cl[0];
#pragma unroll
  for (int b = 1; b < B; ++b) cw = (wave == b) ? cl[b] : cw;
  unsigned long long live[B];
  unsigned lvb = 0;
#pragma unroll
  for (int b = 0; b < B; ++b) {
    cl[b] = (cl[b] <= 0 || cl[b] > LK) ? LK : cl[b];
    live[b] = __builtin_amdgcn_ballot_w64(col * BS < cl[b]);
    lvb |= (col * BS < cl[b]) ? (1u << b) : 0u;
  }
  int ctl[2];
#pragma unroll
  for (int hf = 0; hf < 2; ++hf) {
    int c = cl[4 * hf];
#pragma unroll
    for (int a = 1; a < 4; ++a) c = ((n >> 2) == a) ? cl[4 * hf + a] : c;
    ctl[hf] = c - 4 * g;
  }
  asm volatile("" : "+v"(lvb), "+v"(ctl[0]), "+v"(ctl[1]));
#pragma unroll
  for (int e = 0; e < 4; ++e) {
    lds.u.pro.bt[wave][4 * (lane & 1) + e][lane >> 1] = (int)bt0[e];
    lds.u.pro.bt[wave][4 * (lane & 1) + e][32 + (lane >> 1)] = (int)bt1[e];
  }
  lds_barrier();
  warm(2);

  int pv[B];
#pragma unroll
  for (int b = 0; b < B; ++b) pv[b] = lds.u.pro.bt[b][wave][lane];
  unsigned long long lead[B], heavy[B];
  unsigned eqb = 0;
  {
    unsigned long long eq[B];
#pragma unroll
    for (int b = 0; b < B; ++b) {
      pv[b] = (col * BS < cl[b]) ? pv[b] : (b == 0 ? (int)0x80000000 : pv[b - 1]);
      eq[b] = b == 0 ? 0ull : __builtin_amdgcn_ballot_w64(pv[b] == pv[b - 1]);
      if (b) eqb |= (pv[b] == pv[b - 1]) ? (1u << b) : 0u;
    }
    unsigned long long fol = 0;
#pragma unroll
    for (int b = B - 1; b >= 0; --b) {
      lead[b] = live[b] & ~eq[b];
      heavy[b] = lead[b] & fol;
      fol = eq[b] & (live[b] | fol);
    }
  }
  asm volatile("" : "+v"(eqb));
  auto members = [&](int b) { return (((2u << __builtin_ctz(~(eqb >> (b + 1)))) - 1u) << b) & lvb; };
  int base[B + 1], cnt = 0;
  base[0] = 0;
#pragma unroll
  for (int b = 0; b < B; ++b) {
    const int c = __builtin_popcountll(lead[b]);
    cnt += c;
    base[b + 1] = base[b] + 8 * c + __builtin_popcountll(heavy[b]);
  }
  const int total = base[B];
  const unsigned ta = (unsigned)(total * sp);
  const unsigned mg = h == hs ? 0xFFFFFFFFu / NSS + 1u : 0xFFFFFFFFu / NSF + 1u;
  const int lo = (int)__umulhi(ta, mg) + wave, hi = (int)__umulhi(ta + (unsigned)total, mg) + wave;
  int nmine = 0;
#pragma unroll
  for (int b = 0; b < B; ++b) {
    if (base[b] + 7 < hi && base[b + 1] > lo) {
      const int pos = base[b] + 7 - lo + 8 * mbcnt64(lead[b]) + mbcnt64(heavy[b]);
      const unsigned long long sel = lead[b] & __builtin_amdgcn_ballot_w64((unsigned)pos < (unsigned)(hi - lo));
      if (__builtin_amdgcn_inverse_ballot_w64(sel)) lds.items[wave][nmine + mbcnt64(sel)] = u32x2{(unsigned)pv[b], members(b) | ((unsigned)col << 8)};
      nmine += __builtin_popcountll(sel);
    }
  }
  __builtin_amdgcn_fence(__ATOMIC_RELEASE, "wavefront");
  __builtin_amdgcn_wave_barrier();
  __builtin_amdgcn_fence(__ATOMIC_ACQUIRE, "wavefront");
  u32x2 myit = lds.items[wave][lane < MAX_ITEMS ? lane : 0];

  const unsigned koff = (unsigned)(4 * (n >> 2) * TOK_BYTES + g * 64 + (n & 3) * 16);
  const unsigned voff = (unsigned)(4 * g * TOK_BYTES + n * 16);
  const unsigned hoff = (unsigned)(h * D * 2);
  u32x4 kr[4], vr[4];
  auto issue = [&](u32x4 (&r)[4], const char* src, unsigned off, int i) {
    const unsigned phys = (unsigned)__builtin_amdgcn_readlane((int)myit[0], i);
    const __amdgpu_buffer_rsrc_t rs = make_rsrc(src, i < nmine ? cache_bytes : 0u);
    const unsigned so = phys * (unsigned)PAGE_BYTES + hoff;
#pragma unroll
    for (int t = 0; t < 4; ++t) r[t] = __builtin_amdgcn_raw_buffer_load_b128(rs, off + TOK_BYTES * t, so, AUX_NT);
  };
  issue(kr, kc, koff, 0);
  __builtin_amdgcn_sched_barrier(0);

  myit[1] = lane < nmine ? myit[1] : 0u;
  {
    const unsigned um = wave_or(myit[1] & 0xFFu);
    if (lane == 0) lds.own[wave] = u32x2{um, (unsigned)cnt};
  }
  if (cw <= 0) qv = u32x4{0u, 0u, 0u, 0u};
  lds.u.pro.q[wave * G + g][n] = qv;
  if (wave == 0 && lane == 0) {
    lds.tag = epoch + 1u;
    lds.done = 0u;
    lds.ready = 0u;
    lds.lat = 0u;
    lds.ext = EXT_NONE;
    lds.claim = 0u;
  }
  lds_barrier();
  u32x4 qf[2][4];
#pragma unroll
  for (int hf = 0; hf < 2; ++hf)
#pragma unroll
    for (int s = 0; s < 4; ++s) qf[hf][s] = lds.u.pro.q[hf * 16 + n][4 * g + s];
  const u32x2 ownv = lds.own[lane & 7];
  const unsigned uall = __builtin_amdgcn_readfirstlane(or8(ownv[0]));
  const int nseq = __builtin_popcount(uall);
  const unsigned tag = __builtin_amdgcn_readfirstlane(lds.tag), tg8 = tag << 8;
  const __amdgpu_buffer_rsrc_t rws = make_rsrc(ws, WS_END);
  const unsigned rbase = (unsigned)((hx * ROWS + ix) * SLOTS);

  const float span = 0.125f * PACE * sum8((float)ownv[1]);
  const float dur = fmaxf(span - EARLY_NS * (float)(nseq - 1) - (guest ? GUEST_NS : 0.f), 0.f);
  int del = __builtin_amdgcn_readfirstlane((int)(dur * __builtin_amdgcn_rcpf((float)(2 * nmine + (nmine == 0))))) & -(nmine > 0);
  const unsigned tbase = tstart * 10u + T0_NS;
  unsigned due = tbase + (unsigned)(((wave - LEAD) * del) >> 3);
  unsigned tpoll = tbase + POLL_NS + (unsigned)__builtin_amdgcn_readfirstlane((int)span);
  const unsigned tma = tbase + (unsigned)__builtin_amdgcn_readfirstlane((int)(span * FA));
  const unsigned tmb = tbase + (unsigned)__builtin_amdgcn_readfirstlane((int)(span * FB));
  const bool refwg = ix == REF_A || ix == REF_B;
  const unsigned posto = WS_PROBE + (unsigned)(hx + (ix == REF_B ? 8 : 0)) * 128u;
  auto claim = [&](unsigned bit) {
    unsigned old = 0;
    if (lane == 0) old = __hip_atomic_fetch_or(&lds.claim, bit, WG_RELAXED);
    return (__builtin_amdgcn_readfirstlane(old) & bit) == 0u;
  };
  bool settled = false;
  unsigned tlast = 0, topen = 0;
  auto pace = [&](int left) {
    unsigned now = now_ns();
    if (!settled && (int)(now - tma) >= 0) {
      if (topen) {
        if (lane == 0) __hip_atomic_fetch_add(&lds.lat, min(now - topen - 120u, 2500u) + (1u << 20), WG_RELAXED);
        topen = 0;
      }
      if ((int)(now - tmb) < 0) {
        if (refwg) {
          bool pend;
          for (;;) {
            now = now_ns();
            pend = loads_pending();
            if (!pend || (int)(now - (due + del)) >= 0) break;
            __builtin_amdgcn_s_sleep(1);
          }
          if (pend) topen = tlast | 1u;
          else if (lane == 0) __hip_atomic_fetch_add(&lds.lat, min(now - tlast, 2500u) + (1u << 20), WG_RELAXED);
        }
      } else {
        if (refwg && (int)(now - (tmb + TP_NS)) >= 0 && claim(1u)) {
          const unsigned acc = __hip_atomic_load(&lds.lat, WG_RELAXED);
          __builtin_amdgcn_raw_buffer_store_b128(u32x4{acc, tag, tbase, tag}, rws, (lane >> 3) * 2048 + (lane & 7) * 16, posto, AUX_SC1);
        } else if ((int)(now - (tmb + TC_NS)) >= 0 && claim(2u)) {
          const u32x4 v = __builtin_amdgcn_raw_buffer_load_b128(rws, lane < 16 ? lane * 128u : OOB, WS_PROBE + (unsigned)hx * 2048u, AUX_SC1);
          const bool ok = lane < 16 && v[1] == tag && v[3] == tag;
          const float skew = sum16(ok ? (float)(int)(v[2] - tbase) : 0.f) * __builtin_amdgcn_rcpf(sum16(ok ? 1.f : 0.f));
          const float sum = sum16(ok ? (float)(v[0] & 0xFFFFFu) : 0.f), cn = sum16(ok ? (float)(v[0] >> 20) : 0.f);
          const float e = sum * __builtin_amdgcn_rcpf(cn) - E0_NS;
          const float x = fminf(fmaxf(fminf(e * GX1, XB + e * GX2), XMIN), XMAX) * (span * (1.f / 15200.f)) + fminf(fmaxf(skew * ALIGN, -500.f), 500.f);
          if (lane == 0) __hip_atomic_store(&lds.ext, cn > 0.f ? (int)x : 0, WG_RELAXED);
        }
        const int x = __builtin_amdgcn_readfirstlane(__hip_atomic_load(&lds.ext, WG_RELAXED));
        if (x != EXT_NONE) {
          settled = true;
          tpoll += (unsigned)x;
          del += __builtin_amdgcn_readfirstlane((int)((float)x * __builtin_amdgcn_rcpf((float)left)));
        }
      }
    }
    due += (unsigned)(left <= 2 ? del - SPRINT_NS : del);
    while ((int)((now = now_ns()) - due) < 0) __builtin_amdgcn_s_sleep(1);
    tlast = now;
  };

  auto producers = [&]() {
    if (wave == 0 && lane == 0) __hip_atomic_store((gu32*)(ws + WS_HDR) + bid, tag, DEV_RELAXED);
    int tb = ix >> 2, idx = 0;
    unsigned z = 0;
    asm volatile("" : "+s"(tb), "+v"(z));
#pragma unroll
    for (int a = 0; a < B; ++a) {
      const int ia = base[a] + 7 + 8 * mbcnt64(lead[a], z) + mbcnt64(heavy[a], z);
      idx = __builtin_amdgcn_inverse_ballot_w64(a <= tb ? lead[a] : 0ull) ? ia : idx;
    }
    const int s2 = (int)(((float)((hx == hs ? NSS : NSF) * (idx - wave + 1) - 1) + 0.5f) * __builtin_amdgcn_rcpf((float)total));
    const unsigned bit = ((lvb >> tb) & 1u) << (s2 & 31), blo = s2 < 32 ? bit : 0u, bhi = s2 < 32 ? 0u : bit;
    const bool shr = ((eqb >> tb) & 1u) || members(tb) != (1u << tb);
    const u32x4 r4 = {wave_or(blo), wave_or(bhi), wave_or(shr ? blo : 0u), wave_or(shr ? bhi : 0u)};
    if (lane == 0) {
      lds.prod[wave] = r4;
      asm volatile("s_waitcnt lgkmcnt(0)" ::: "memory");
      __hip_atomic_fetch_add(&lds.ready, 1u, WG_RELAXED);
    }
  };
  unsigned long long need = 0;
  int nsrc = 0, srcv = 0, rowv = 0;
  unsigned go[2] = {0u, 0u};
  auto slot_off = [&](int k) { return (rbase + (unsigned)__builtin_amdgcn_ds_bpermute(4 * k, srcv)) * 1024u + (lane & 7) * 16u; };
  auto resolve = [&]() {
    while (__builtin_amdgcn_readfirstlane(__hip_atomic_load(&lds.ready, WG_RELAXED)) < NW) __builtin_amdgcn_s_sleep(1);
    asm volatile("" ::: "memory");
    const u32x4 pr = lds.prod[lane & 7];
    need = or8_u64(pr[0], pr[1]);
    const unsigned long long early = or8_u64(pr[2], pr[3]);
    const int seqv = __builtin_amdgcn_ds_permute(lane < B && ((uall >> lane) & 1u) ? 4 * __builtin_popcount(uall & ((1u << (lane & 7)) - 1u)) : 252, lane);
    rowv = 4 * __builtin_amdgcn_ds_bpermute(4 * (lane >> 3), seqv) + (wave & 3);
    nsrc = __builtin_popcountll(need);
    srcv = __builtin_amdgcn_ds_permute(__builtin_amdgcn_inverse_ballot_w64(need) ? 4 * mbcnt64(need) : 252, lane);
#pragma unroll
    for (int t = 0; t < 2; ++t) go[t] = slot_off(8 * t + (lane >> 3));
    return __builtin_amdgcn_inverse_ballot_w64(need & ~early) ? WS_PO + (rbase + (unsigned)lane) * 1024u + wave * 128u : WS_END;
  };
  const int ires = min(TOUCH_AT, nmine - 1);

  f32x4 o[2][8];
  float m[2], l[2];
#pragma unroll
  for (int hf = 0; hf < 2; ++hf) {
    m[hf] = MINIT;
    l[hf] = 0.f;
#pragma unroll
    for (int j = 0; j < 8; ++j) o[hf][j] = f32x4{0.f, 0.f, 0.f, 0.f};
  }
  unsigned tch = 0;
  int i = 0;
  asm volatile("" : "+s"(i));
  do {
    pace(2 * (nmine - i) - 1);
    issue(vr, vc, voff, i);
    __builtin_amdgcn_sched_barrier(0);
    if (i == 0) producers();
    const unsigned meta = (unsigned)__builtin_amdgcn_readlane((int)myit[1], i);
    const int tok0 = (int)(meta >> 8) * BS;
    u32x4 t0, t1, t2, t3, kt[4];
    asm volatile("" ::"v"(tch));
    xchg<0>(kr[0], kr[1], t0, t1);
    xchg<0>(kr[2], kr[3], t2, t3);
    xchg<1>(t0, t2, kt[0], kt[2]);
    xchg<1>(t1, t3, kt[1], kt[3]);
    __builtin_amdgcn_sched_barrier(0);
    unsigned tcho = (refwg && wave == NW - 1 && i == 0 && lane < 8) ? posto + lane * 2048u : WS_END;
    if (i >= ires && nsrc == 0) tcho = resolve();
    if (i + 1 < nmine) pace(2 * (nmine - i) - 2);
    issue(kr, kc, koff, i + 1);
    tch = __builtin_amdgcn_raw_buffer_load_b32(rws, tcho, 0, AUX_SC1);
    __builtin_amdgcn_sched_barrier(0);
    u32x2 pp[2];
#pragma unroll
    for (int hf = 0; hf < 2; ++hf) {
      if ((meta >> (4 * hf)) & 0xFu) {
        f32x4 s = {0.f, 0.f, 0.f, 0.f};
#pragma unroll
        for (int st = 0; st < 4; ++st)
          s = __builtin_amdgcn_mfma_f32_16x16x32_bf16(__builtin_bit_cast(bf16x8, kt[st]), __builtin_bit_cast(bf16x8, qf[hf][st]), s, 0, 0, 0);
        const int lim = ((meta >> (4 * hf + (n >> 2))) & 1u) ? ctl[hf] - tok0 : 0;
        float x[4], e[4];
#pragma unroll
        for (int r = 0; r < 4; ++r) x[r] = (r < lim) ? s[r] * SCL : -INFINITY;
        const float tm = xrow_max(fmaxf(fmaxf(x[0], x[1]), fmaxf(x[2], x[3])));
        const float mn = fmaxf(m[hf], tm);
        const float al = __builtin_amdgcn_exp2f(m[hf] - mn);
#pragma unroll
        for (int r = 0; r < 4; ++r) e[r] = __builtin_amdgcn_exp2f(x[r] - mn);
        l[hf] = l[hf] * al + ((e[0] + e[1]) + (e[2] + e[3]));
        m[hf] = mn;
#pragma unroll
        for (int j = 0; j < 8; ++j) o[hf][j] *= al;
        pp[hf] = u32x2{pack_bf16(e[0], e[1]), pack_bf16(e[2], e[3])};
      }
    }
    if (i + 1 >= nmine) {
#pragma unroll
      for (int hf = 0; hf < 2; ++hf) {
        const float lt = xrow_sum(l[hf]);
        if (g == 0) lds.ml[wave][hf * 16 + n] = f32x2{m[hf], lt};
      }
      asm volatile("s_waitcnt lgkmcnt(0)" ::: "memory");
      if (lane == 0) __hip_atomic_fetch_add(&lds.done, 1u, WG_RELAXED);
    }
    __builtin_amdgcn_sched_barrier(0);
    u32x2 vt[8];
#pragma unroll
    for (int w = 0; w < 4; ++w) {
      vt[2 * w] = u32x2{__builtin_amdgcn_perm(vr[1][w], vr[0][w], 0x05040100u), __builtin_amdgcn_perm(vr[3][w], vr[2][w], 0x05040100u)};
      vt[2 * w + 1] = u32x2{__builtin_amdgcn_perm(vr[1][w], vr[0][w], 0x07060302u), __builtin_amdgcn_perm(vr[3][w], vr[2][w], 0x07060302u)};
    }
#pragma unroll
    for (int hf = 0; hf < 2; ++hf) {
      if ((meta >> (4 * hf)) & 0xFu) {
#pragma unroll
        for (int j = 0; j < 8; ++j)
          o[hf][j] = __builtin_amdgcn_mfma_f32_16x16x16bf16_1k(__builtin_bit_cast(s16x4, vt[j]), __builtin_bit_cast(s16x4, pp[hf]), o[hf][j], 0, 0, 0);
      }
    }
    __builtin_amdgcn_sched_barrier(0);
  } while (++i < nmine);

#pragma unroll
  for (int hf = 0; hf < 2; ++hf) {
    const int row = hf * 16 + n;
    if ((uall >> (row >> 2)) & 1u) {
#pragma unroll
      for (int j = 0; j < 8; ++j) *reinterpret_cast<f32x4*>(&lds.u.o[wave][row][16 * j + 4 * g]) = o[hf][j];
    }
  }
  __builtin_amdgcn_s_waitcnt(0x0F70);
  asm volatile("" ::"v"(cpf), "v"(pf[0]), "v"(pf[1]), "v"(pf[2]), "v"(pf[3]), "v"(pf[4]), "v"(pf[5]));

  const int kstep = nseq > 2 ? 2 : 1, kfirst = wave >> 2;
  if (kfirst < kstep) {
    while (__builtin_amdgcn_readfirstlane(__hip_atomic_load(&lds.done, WG_RELAXED)) < NW) __builtin_amdgcn_s_sleep(1);
    asm volatile("" ::: "memory");
    const f32x2 ml = lds.ml[lane & 7][rowv & 31];
    const float M = max8(ml[0]);
    const float wt = __builtin_amdgcn_exp2f(ml[0] - M);
    const float L = sum8(wt * ml[1]);
    const int wn = (int)__float_as_uint(wt * __builtin_amdgcn_rcpf(L));
    const int lsev = (int)__float_as_uint(M + __builtin_amdgcn_logf(L));
    for (int k = kfirst;; k += kstep) {
      const int row = __builtin_amdgcn_readlane(rowv, 8 * k) & 31;
      f32x2 wp[4];
#pragma unroll
      for (int w = 0; w < NW; ++w) wp[w >> 1][w & 1] = __uint_as_float((unsigned)__builtin_amdgcn_readlane(wn, 8 * k + w));
      const unsigned lh = (unsigned)__builtin_amdgcn_readlane(lsev, 8 * k) >> (16 * (lane & 1));
      unsigned tw0 = tg8 | (lh & 0xFFu), tw1 = tg8 | ((lh >> 8) & 0xFFu);
      int e0 = (row * OROW + 2 * lane) * 4, e1 = e0 + 16 * ROWS * OROW;
      unsigned pso = WS_PO + (unsigned)((h * ROWS + row) * SLOTS + sp) * 1024u;
      asm volatile("" : "+v"(tw0), "+v"(tw1), "+v"(e0), "+v"(e1), "+s"(pso), "+v"(wp[0]), "+v"(wp[1]), "+v"(wp[2]), "+v"(wp[3]));
      if (k == kfirst) lds_barrier();
      if (k >= nseq) break;
      const char* img = reinterpret_cast<const char*>(&lds.u.o[0][0][0]);
      f32x2 ov[NW];
#pragma unroll
      for (int w = 0; w < NW; ++w) ov[w] = *reinterpret_cast<const f32x2*>(img + (w < 4 ? e0 : e1) + (w & 3) * (ROWS * OROW * 4));
      f32x2 a0 = wp[0][0] * ov[0], a1 = wp[2][0] * ov[4];
#pragma unroll
      for (int w = 1; w < 4; ++w) {
        a0 += wp[w >> 1][w & 1] * ov[w];
        a1 += wp[2 + (w >> 1)][w & 1] * ov[4 + w];
      }
      a0 += a1;
      __builtin_amdgcn_raw_buffer_store_b128(u32x4{__float_as_uint(a0[0]), tw0, __float_as_uint(a0[1]), tw1}, rws, lane * 16, pso, AUX_SC1);
      if (k + kstep >= nseq) break;
    }
  } else {
    lds_barrier();
  }
  __builtin_amdgcn_sched_barrier(0);

  if (bid == 0 && wave == NW - 2) {
    for (unsigned spins = 0;; ++spins) {
      const u32x4 hd = __builtin_amdgcn_raw_buffer_load_b128(rws, lane * 16, WS_HDR, AUX_SC1);
      const bool ok = hd[0] == tag && hd[1] == tag && hd[2] == tag && hd[3] == tag;
      if (__builtin_amdgcn_ballot_w64(!ok) == 0ull || spins > SPIN_CAP) break;
      __builtin_amdgcn_s_sleep(2);
      asm volatile("" ::: "memory");
    }
    if (lane == 0) __hip_atomic_store((gu32*)(ws + WS_EPOCH), tag, DEV_RELAXED);
  }

  u32x4 og[2];
  auto gather_init = [&](int k0) {
#pragma unroll
    for (int t = 0; t < 2; ++t) {
      const unsigned tw = (k0 + 8 * t + (lane >> 3) < nsrc) ? ~tg8 : tg8;
      og[t] = u32x4{0u, tw | ((lane & 1) ? 0x80u : 0u), 0u, tw | ((lane & 1) ? 0xFFu : 0u)};
    }
  };
  gather_init(0);
  unsigned psel = (lane & 1) ? 0x04000c0cu : 0x0c0c0400u;
  const unsigned oo = (32u * ((lane >> 1) & 3) + 16u * (lane & 1) + 8u * (lane >> 5) + wave) * 2u;
  unsigned long long op = (unsigned long long)out + (unsigned)(((ix >> 2) * H + hx * G + (ix & 3)) * D * 2) + oo;
  unsigned gso = WS_PO + wave * 128u;
  asm volatile("" : "+v"(psel), "+v"(op), "+v"(og[0]), "+v"(og[1]), "+s"(gso));
  __builtin_amdgcn_sched_barrier(0);
  if (!guest && ((need >> sp) & 1u)) __builtin_amdgcn_s_sleep(10);
  while ((int)(now_ns() - tpoll) < 0) __builtin_amdgcn_s_sleep(1);
#pragma unroll
  for (int t = 0; t < 2; ++t)
    if (8 * t + (lane >> 3) < nsrc) og[t] = __builtin_amdgcn_raw_buffer_load_b128(rws, go[t], gso, AUX_SC1);
  float Mr, Wr;
  f32x2 ar;
  auto batch = [&](bool first) {
    auto stale = [&](const u32x4& v) { return (v[1] ^ tg8) | (v[3] ^ tg8); };
    if (__builtin_expect(__builtin_amdgcn_ballot_w64((stale(og[0]) | stale(og[1])) > 0xFFu) != 0ull, 0)) {
      unsigned spins = 0;
      do {
        __builtin_amdgcn_s_sleep(3);
        asm volatile("" ::: "memory");
        if (stale(og[0]) > 0xFFu) og[0] = __builtin_amdgcn_raw_buffer_load_b128(rws, go[0], gso, AUX_SC1);
        if (stale(og[1]) > 0xFFu) og[1] = __builtin_amdgcn_raw_buffer_load_b128(rws, go[1], gso, AUX_SC1);
      } while (__builtin_amdgcn_ballot_w64((stale(og[0]) | stale(og[1])) > 0xFFu) != 0ull && ++spins < SPIN_CAP);
    }
    const unsigned h0 = __builtin_amdgcn_perm(og[0][3], og[0][1], psel), h1 = __builtin_amdgcn_perm(og[1][3], og[1][1], psel);
    const float l0 = __uint_as_float(h0 | dpp<XOR1>(h0)), l1 = __uint_as_float(h1 | dpp<XOR1>(h1));
    const float M = group_max(l0, first ? l1 : fmaxf(l1, Mr));
    const float w0 = __builtin_amdgcn_exp2f(l0 - M), w1 = __builtin_amdgcn_exp2f(l1 - M);
    const float wsum = w0 + w1;
    const f32x2 part = {fmaf(w0, __uint_as_float(og[0][0]), w1 * __uint_as_float(og[1][0])), fmaf(w0, __uint_as_float(og[0][2]), w1 * __uint_as_float(og[1][2]))};
    if (first) {
      Wr = wsum;
      ar = part;
    } else {
      const float wr = __builtin_amdgcn_exp2f(Mr - M);
      Wr = Wr * wr + wsum;
      ar = ar * wr + part;
    }
    Mr = M;
  };
  batch(true);
  for (int k0 = 16; __builtin_expect(k0 < nsrc, 0); k0 += 16) {
    gather_init(k0);
#pragma unroll
    for (int t = 0; t < 2; ++t) go[t] = slot_off(k0 + 8 * t + (lane >> 3));
    batch(false);
  }
  const auto sw = __builtin_amdgcn_permlane32_swap(__float_as_uint(ar[0]), __float_as_uint(ar[1]), false, false);
  const auto ww = __builtin_amdgcn_permlane32_swap(__float_as_uint(Wr), __float_as_uint(Wr), false, false);
  const float z0 = __uint_as_float(sw[0]) + __uint_as_float(sw[1]), w2 = __uint_as_float(ww[0]) + __uint_as_float(ww[1]);
  const auto s16 = __builtin_amdgcn_permlane16_swap(__float_as_uint(z0), __float_as_uint(w2), false, false);
  float y = __uint_as_float(s16[0]) + __uint_as_float(s16[1]);
  y += dppf<ROR8>(y);
  const auto f16 = __builtin_amdgcn_permlane16_swap(__float_as_uint(y), __float_as_uint(y), false, false);
  const unsigned pk = pack_bf16(__uint_as_float(f16[0]) * __builtin_amdgcn_rcpf(__uint_as_float(f16[1])), 0.f);
  if (__builtin_amdgcn_inverse_ballot_w64(0x000000FF000000FFull)) *(__attribute__((address_space(1))) unsigned short*)op = (unsigned short)pk;
}

}

extern "C" void kernel_launch(void* const* d_in, const int* in_sizes, int n_in, void* d_out, int out_size, void* d_ws,
                              size_t ws_size, hipStream_t stream) {
  const unsigned cache_bytes = (unsigned)in_sizes[1] * 2u;
  attn_paged<<<NWG, NT, 0, stream>>>((const __hip_bfloat16*)d_in[0], (const char*)d_in[1], (const char*)d_in[2],
                                     (const int*)d_in[3], (const int*)d_in[4], (__hip_bfloat16*)d_out, (char*)d_ws,
                                     cache_bytes);
}
```

```cpp
#include <hip/hip_runtime.h>
#include <hip/hip_bf16.h>
#include <cstdint>

namespace {

constexpr int B = 8, H = 32, HKV = 8, G = H / HKV, D = 128, BS = 16, NB = 512, LK = NB * BS;
constexpr int NWG = 256, NW = 8, NT = NW * 64;
constexpr int ROWS = B * G;
constexpr int PAGE_BYTES = BS * HKV * D * 2;
constexpr int TOK_BYTES = HKV * D * 2;
constexpr float SCL = 0.08838834764831845f * 1.4426950408889634f;
constexpr float MINIT = -1e30f;

constexpr int GUESTS = 2;
constexpr int NSF = 32 - GUESTS, NSS = 32 + 3 * GUESTS;
constexpr int SLOTS = 48;
constexpr int MAX_ITEMS = 24;
constexpr int WARM = 2;
constexpr int CODE_LINES = 218;

#define PACE 66.5f
#define T0_NS 2400
#define EARLY_NS 290.0f
#define GUEST_NS 300.0f
#define SPRINT_NS 170
#define POLL_NS 650
#define FA 0.23f
#define FB 0.36f
#define ALIGN 1.0f
#define TP_NS 300
#define TC_NS 1300
#define E0_NS 775.0f
#define GX1 5.0f
#define XB 450.0f
#define GX2 2.5f
#define XMIN 0.0f
#define XMAX 2300.0f
constexpr int LEAD = 4, TOUCH_AT = 3, REF_A = 14, REF_B = 23;
constexpr unsigned SPIN_CAP = 1u << 20;

constexpr unsigned WS_EPOCH = 0;
constexpr unsigned WS_HDR = 4096;
constexpr unsigned WS_PROBE = 8192;
constexpr unsigned WS_PO = WS_PROBE + 128 * 128;
constexpr unsigned WS_END = WS_PO + HKV * ROWS * SLOTS * 1024;

constexpr int AUX_NT = 2, AUX_SC1 = 16;
constexpr unsigned OOB = 0x80000000u;

using u32x4 = __attribute__((ext_vector_type(4))) unsigned;
using u32x2 = __attribute__((ext_vector_type(2))) unsigned;
using f32x4 = __attribute__((ext_vector_type(4))) float;
using f32x2 = __attribute__((ext_vector_type(2))) float;
using bf16x8 = __attribute__((ext_vector_type(8))) __bf16;
using bf16x2 = __attribute__((ext_vector_type(2))) __bf16;
using s16x4 = __attribute__((ext_vector_type(4))) short;
typedef __attribute__((address_space(1))) unsigned gu32;

constexpr int OROW = D + 4, BTW = 68, QROW = 17;
struct Lds {
  union {
    struct {
      u32x4 q[ROWS][QROW];
      int bt[B][NW][BTW];
    } pro;
    float o[NW][ROWS][OROW];
  } u;
  f32x2 ml[NW][ROWS];
  u32x2 items[NW][MAX_ITEMS];
  u32x4 prod[NW];
  u32x2 own[NW];
  unsigned tag;
  unsigned done;
  unsigned ready;
  unsigned lat;
  int ext;
  unsigned claim;
  unsigned pad[2];
};
constexpr int EXT_NONE = -0x40000000;

#define WG_RELAXED __ATOMIC_RELAXED, __HIP_MEMORY_SCOPE_WORKGROUP
#define DEV_RELAXED __ATOMIC_RELAXED, __HIP_MEMORY_SCOPE_AGENT

__device__ __forceinline__ __amdgpu_buffer_rsrc_t make_rsrc(const void* p, unsigned bytes) {
  return __builtin_amdgcn_make_buffer_rsrc((void*)p, 0, bytes, 0x00020000);
}
__device__ __forceinline__ void lds_barrier() { asm volatile("s_waitcnt lgkmcnt(0)\n\ts_barrier" ::: "memory"); }
__device__ __forceinline__ unsigned now_ns() { return (unsigned)__builtin_amdgcn_s_memrealtime() * 10u; }
__device__ __forceinline__ bool loads_pending() { return (__builtin_amdgcn_s_getreg(7 | (3 << 11)) & 15) != 0; }

template <int CTRL>
__device__ __forceinline__ unsigned dpp(unsigned x) {
  return (unsigned)__builtin_amdgcn_mov_dpp((int)x, CTRL, 0xf, 0xf, true);
}
template <int CTRL>
__device__ __forceinline__ float dppf(float x) {
  return __uint_as_float(dpp<CTRL>(__float_as_uint(x)));
}
constexpr int XOR1 = 0xB1, XOR2 = 0x4E, HMIR = 0x141, ROR8 = 0x128;

__device__ __forceinline__ float xrow_max(float x) {
  auto s = __builtin_amdgcn_permlane16_swap(__float_as_uint(x), __float_as_uint(x), false, false);
  x = fmaxf(__uint_as_float(s[0]), __uint_as_float(s[1]));
  auto t = __builtin_amdgcn_permlane32_swap(__float_as_uint(x), __float_as_uint(x), false, false);
  return fmaxf(__uint_as_float(t[0]), __uint_as_float(t[1]));
}
__device__ __forceinline__ float xrow_sum(float x) {
  auto s = __builtin_amdgcn_permlane16_swap(__float_as_uint(x), __float_as_uint(x), false, false);
  x = __uint_as_float(s[0]) + __uint_as_float(s[1]);
  auto t = __builtin_amdgcn_permlane32_swap(__float_as_uint(x), __float_as_uint(x), false, false);
  return __uint_as_float(t[0]) + __uint_as_float(t[1]);
}
__device__ __forceinline__ float group_max(float a, float b) {
  float r, t;
  asm volatile(
      "v_max_f32 %0, %2, %3\n\ts_nop 1\n\t"
      "v_max_f32_dpp %0, %0, %0 row_ror:8 row_mask:0xf bank_mask:0xf\n\t"
      "v_mov_b32 %1, %0\n\ts_nop 1\n\t"
      "v_permlane16_swap_b32 %0, %1\n\t"
      "v_max_f32 %0, %0, %1\n\t"
      "v_mov_b32 %1, %0\n\ts_nop 1\n\t"
      "v_permlane32_swap_b32 %0, %1\n\t"
      "v_max_f32 %0, %0, %1"
      : "=&v"(r), "=&v"(t)
      : "v"(a), "v"(b));
  return r;
}
__device__ __forceinline__ float max8(float x) {
  x = fmaxf(x, dppf<XOR1>(x));
  x = fmaxf(x, dppf<XOR2>(x));
  return fmaxf(x, dppf<HMIR>(x));
}
__device__ __forceinline__ float sum8(float x) {
  x += dppf<XOR1>(x);
  x += dppf<XOR2>(x);
  return x + dppf<HMIR>(x);
}
__device__ __forceinline__ float sum16(float x) {
  x = sum8(x);
  return x + dppf<ROR8>(x);
}
__device__ __forceinline__ unsigned or8(unsigned x) {
  x |= dpp<XOR1>(x);
  x |= dpp<XOR2>(x);
  return x | dpp<HMIR>(x);
}
__device__ __forceinline__ unsigned wave_or(unsigned x) {
  x = or8(x);
  x |= dpp<ROR8>(x);
  auto s = __builtin_amdgcn_permlane16_swap(x, x, false, false);
  x = s[0] | s[1];
  auto t = __builtin_amdgcn_permlane32_swap(x, x, false, false);
  return t[0] | t[1];
}
__device__ __forceinline__ unsigned long long or8_u64(unsigned lo, unsigned hi) {
  return (unsigned)__builtin_amdgcn_readfirstlane(or8(lo)) | ((unsigned long long)(unsigned)__builtin_amdgcn_readfirstlane(or8(hi)) << 32);
}
__device__ __forceinline__ unsigned pack_bf16(float lo, float hi) {
  return __builtin_bit_cast(unsigned, __builtin_convertvector(f32x2{lo, hi}, bf16x2));
}
__device__ __forceinline__ int mbcnt64(unsigned long long m, unsigned z = 0u) {
  return (int)__builtin_amdgcn_mbcnt_hi((unsigned)(m >> 32), __builtin_amdgcn_mbcnt_lo((unsigned)m, z));
}

#define XCHG_BODY(QP)                                                                 \
  "v_cndmask_b32_dpp %0, %12, %8, vcc " QP " row_mask:0xf bank_mask:0xf\n\t"          \
  "v_cndmask_b32_dpp %1, %13, %9, vcc " QP " row_mask:0xf bank_mask:0xf\n\t"          \
  "v_cndmask_b32_dpp %2, %14, %10, vcc " QP " row_mask:0xf bank_mask:0xf\n\t"         \
  "v_cndmask_b32_dpp %3, %15, %11, vcc " QP " row_mask:0xf bank_mask:0xf\n\t"         \
  "s_not_b64 vcc, vcc\n\t"                                                            \
  "v_cndmask_b32_dpp %4, %8, %12, vcc " QP " row_mask:0xf bank_mask:0xf\n\t"          \
  "v_cndmask_b32_dpp %5, %9, %13, vcc " QP " row_mask:0xf bank_mask:0xf\n\t"          \
  "v_cndmask_b32_dpp %6, %10, %14, vcc " QP " row_mask:0xf bank_mask:0xf\n\t"         \
  "v_cndmask_b32_dpp %7, %11, %15, vcc " QP " row_mask:0xf bank_mask:0xf"
#define XCHG_OPS                                                                              \
  : "=&v"(x0), "=&v"(x1), "=&v"(x2), "=&v"(x3), "=&v"(y0), "=&v"(y1), "=&v"(y2), "=&v"(y3)    \
  : "v"(a[0]), "v"(a[1]), "v"(a[2]), "v"(a[3]), "v"(b[0]), "v"(b[1]), "v"(b[2]), "v"(b[3])    \
  : "vcc", "scc"
template <int BIT>
__device__ __forceinline__ void xchg(const u32x4& a, const u32x4& b, u32x4& x, u32x4& y) {
  unsigned x0, x1, x2, x3, y0, y1, y2, y3;
  if constexpr (BIT == 0)
    asm volatile("s_mov_b32 vcc_lo, 0x55555555\n\ts_mov_b32 vcc_hi, 0x55555555\n\t" XCHG_BODY("quad_perm:[1,0,3,2]") XCHG_OPS);
  else
    asm volatile("s_mov_b32 vcc_lo, 0x33333333\n\ts_mov_b32 vcc_hi, 0x33333333\n\t" XCHG_BODY("quad_perm:[2,3,0,1]") "\n\ts_nop 1" XCHG_OPS);
  x = u32x4{x0, x1, x2, x3};
  y = u32x4{y0, y1, y2, y3};
}

__global__ void __launch_bounds__(NT, 2)
attn_paged(const __hip_bfloat16* __restrict__ q, const char* __restrict__ kc, const char* __restrict__ vc,
           const int* __restrict__ bt, const int* __restrict__ ctx, __hip_bfloat16* __restrict__ out, char* ws,
           unsigned cache_bytes) {
  __shared__ __attribute__((aligned(16))) Lds lds;
  const int tid = threadIdx.x, lane = tid & 63;
  const int wave = __builtin_amdgcn_readfirstlane(tid >> 6);
  const int n = lane & 15, g = lane >> 4;
  const int bid = blockIdx.x;
  const int hx = bid & 7, ix = bid >> 3;
  const int col = NW * lane + wave;

  const unsigned tstart = (unsigned)__builtin_amdgcn_s_memrealtime();
  unsigned cpf;
  {
    unsigned long long pc;
    asm volatile("s_getpc_b64 %0" : "=s"(pc));
    cpf = *(const gu32*)((pc & ~63ull) + (tid < CODE_LINES ? tid : 0) * 64u);
  }
  unsigned bto = (unsigned)(wave * NB * 4 + lane * 16);
  unsigned wvo = (unsigned)((lane >> 5) * PAGE_BYTES + ((lane & 31) >> 1) * TOK_BYTES + (lane & 1) * 128);
  unsigned wso = (unsigned)(ix * 3 * WARM) * (unsigned)PAGE_BYTES + (unsigned)(hx * D * 2);
  asm volatile("" : "+v"(bto), "+v"(wvo), "+s"(wso));
  asm volatile("" ::"s"(cache_bytes), "s"(kc), "s"(out));
  __builtin_amdgcn_sched_barrier(0);

  const u32x4 bt0 = *reinterpret_cast<const u32x4*>(reinterpret_cast<const char*>(bt) + bto);
  const u32x4 bt1 = *reinterpret_cast<const u32x4*>(reinterpret_cast<const char*>(bt) + bto + 1024);
  int cl[B];
#pragma unroll
  for (int b = 0; b < B; ++b) cl[b] = ctx[b];
  __builtin_amdgcn_sched_barrier(0);
  asm volatile("s_barrier" ::: "memory");

  unsigned pf[6];
  auto warm = [&](int slot) {
    const unsigned nrec = wave == NW - 1 ? cache_bytes : 0u, so = wso + (unsigned)(slot * WARM * PAGE_BYTES);
    pf[2 * slot] = __builtin_amdgcn_raw_buffer_load_b32(make_rsrc(kc, nrec), wvo, so, 0);
    pf[2 * slot + 1] = __builtin_amdgcn_raw_buffer_load_b32(make_rsrc(vc, nrec), wvo, so, 0);
    __builtin_amdgcn_sched_barrier(0);
  };
  static_assert(WARM == 2, "one load instruction covers two pages");
  warm(0);
  const int hs = (hx & 4) | ((1 - (int)((size_t)kc >> 8)) & 3);
  const bool guest = hx != hs && ix >= NSF;
  int h = guest ? hs : hx;
  const int spg = ((hx - hs - 1) & 3) * GUESTS - NSF, spr = hx == hs ? 3 * GUESTS : 0;
  int sp = ix + (guest ? spg : spr);
  asm volatile("" : "+s"(h), "+s"(sp));
  u32x4 qv = reinterpret_cast<const u32x4*>(q + (size_t)(wave * H + h * G) * D)[lane];
  const unsigned epoch = __hip_atomic_load((gu32*)(ws + WS_EPOCH), DEV_RELAXED);
  __builtin_amdgcn_sched_barrier(0);
  asm volatile("" ::"s"(cl[0]));
  __builtin_amdgcn_sched_barrier(0);
  warm(1);
  asm volatile("" : "+s"(cl[0]), "+s"(cl[1]), "+s"(cl[2]), "+s"(cl[3]), "+s"(cl[4]), "+s"(cl[5]), "+s"(cl[6]), "+s"(cl[7]));

  int cw = cl[0];
#pragma unroll
  for (int b = 1; b < B; ++b) cw = (wave == b) ? cl[b] : cw;
  unsigned long long live[B];
  unsigned lvb = 0;
#pragma unroll
  for (int b = 0; b < B; ++b) {
    cl[b] = (cl[b] <= 0 || cl[b] > LK) ? LK : cl[b];
    live[b] = __builtin_amdgcn_ballot_w64(col * BS < cl[b]);
    lvb |= (col * BS < cl[b]) ? (1u << b) : 0u;
  }
  int ctl[2];
#pragma unroll
  for (int hf = 0; hf < 2; ++hf) {
    int c = cl[4 * hf];
#pragma unroll
    for (int a = 1; a < 4; ++a) c = ((n >> 2) == a) ? cl[4 * hf + a] : c;
    ctl[hf] = c - 4 * g;
  }
  asm volatile("" : "+v"(lvb), "+v"(ctl[0]), "+v"(ctl[1]));
#pragma unroll
  for (int e = 0; e < 4; ++e) {
    lds.u.pro.bt[wave][4 * (lane & 1) + e][lane >> 1] = (int)bt0[e];
    lds.u.pro.bt[wave][4 * (lane & 1) + e][32 + (lane >> 1)] = (int)bt1[e];
  }
  lds_barrier();
  warm(2);

  int pv[B];
#pragma unroll
  for (int b = 0; b < B; ++b) pv[b] = lds.u.pro.bt[b][wave][lane];
  unsigned long long lead[B], heavy[B];
  unsigned eqb = 0;
  {
    unsigned long long eq[B];
#pragma unroll
    for (int b = 0; b < B; ++b) {
      pv[b] = (col * BS < cl[b]) ? pv[b] : (b == 0 ? (int)0x80000000 : pv[b - 1]);
      eq[b] = b == 0 ? 0ull : __builtin_amdgcn_ballot_w64(pv[b] == pv[b - 1]);
      if (b) eqb |= (pv[b] == pv[b - 1]) ? (1u << b) : 0u;
    }
    unsigned long long fol = 0;
#pragma unroll
    for (int b = B - 1; b >= 0; --b) {
      lead[b] = live[b] & ~eq[b];
      heavy[b] = lead[b] & fol;
      fol = eq[b] & (live[b] | fol);
    }
  }
  asm volatile("" : "+v"(eqb));
  auto members = [&](int b) { return (((2u << __builtin_ctz(~(eqb >> (b + 1)))) - 1u) << b) & lvb; };
  int base[B + 1], cnt = 0;
  base[0] = 0;
#pragma unroll
  for (int b = 0; b < B; ++b) {
    const int c = __builtin_popcountll(lead[b]);
    cnt += c;
    base[b + 1] = base[b] + 8 * c + __builtin_popcountll(heavy[b]);
  }
  const int total = base[B];
  const unsigned ta = (unsigned)(total * sp);
  const unsigned mg = h == hs ? 0xFFFFFFFFu / NSS + 1u : 0xFFFFFFFFu / NSF + 1u;
  const int lo = (int)__umulhi(ta, mg) + wave, hi = (int)__umulhi(ta + (unsigned)total, mg) + wave;
  int nmine = 0;
#pragma unroll
  for (int b = 0; b < B; ++b) {
    if (base[b] + 7 < hi && base[b + 1] > lo) {
      const int pos = base[b] + 7 - lo + 8 * mbcnt64(lead[b]) + mbcnt64(heavy[b]);
      const unsigned long long sel = lead[b] & __builtin_amdgcn_ballot_w64((unsigned)pos < (unsigned)(hi - lo));
      if (__builtin_amdgcn_inverse_ballot_w64(sel)) lds.items[wave][nmine + mbcnt64(sel)] = u32x2{(unsigned)pv[b], members(b) | ((unsigned)col << 8)};
      nmine += __builtin_popcountll(sel);
    }
  }
  __builtin_amdgcn_fence(__ATOMIC_RELEASE, "wavefront");
  __builtin_amdgcn_wave_barrier();
  __builtin_amdgcn_fence(__ATOMIC_ACQUIRE, "wavefront");
  u32x2 myit = lds.items[wave][lane < MAX_ITEMS ? lane : 0];

  const unsigned koff = (unsigned)(4 * (n >> 2) * TOK_BYTES + g * 64 + (n & 3) * 16);
  const unsigned voff = (unsigned)(4 * g * TOK_BYTES + n * 16);
  const unsigned hoff = (unsigned)(h * D * 2);
  u32x4 kr[4], vr[4];
  auto issue = [&](u32x4 (&r)[4], const char* src, unsigned off, int i) {
    const unsigned phys = (unsigned)__builtin_amdgcn_readlane((int)myit[0], i);
    const __amdgpu_buffer_rsrc_t rs = make_rsrc(src, i < nmine ? cache_bytes : 0u);
    const unsigned so = phys * (unsigned)PAGE_BYTES + hoff;
#pragma unroll
    for (int t = 0; t < 4; ++t) r[t] = __builtin_amdgcn_raw_buffer_load_b128(rs, off + TOK_BYTES * t, so, AUX_NT);
  };
  issue(kr, kc, koff, 0);
  __builtin_amdgcn_sched_barrier(0);

  myit[1] = lane < nmine ? myit[1] : 0u;
  {
    const unsigned um = wave_or(myit[1] & 0xFFu);
    if (lane == 0) lds.own[wave] = u32x2{um, (unsigned)cnt};
  }
  if (cw <= 0) qv = u32x4{0u, 0u, 0u, 0u};
  lds.u.pro.q[wave * G + g][n] = qv;
  if (wave == 0 && lane == 0) {
    lds.tag = epoch + 1u;
    lds.done = 0u;
    lds.ready = 0u;
    lds.lat = 0u;
    lds.ext = EXT_NONE;
    lds.claim = 0u;
  }
  lds_barrier();
  u32x4 qf[2][4];
#pragma unroll
  for (int hf = 0; hf < 2; ++hf)
#pragma unroll
    for (int s = 0; s < 4; ++s) qf[hf][s] = lds.u.pro.q[hf * 16 + n][4 * g + s];
  const u32x2 ownv = lds.own[lane & 7];
  const unsigned uall = __builtin_amdgcn_readfirstlane(or8(ownv[0]));
  const int nseq = __builtin_popcount(uall);
  const unsigned tag = __builtin_amdgcn_readfirstlane(lds.tag), tg8 = tag << 8;
  const __amdgpu_buffer_rsrc_t rws = make_rsrc(ws, WS_END);
  const unsigned rbase = (unsigned)((hx * ROWS + ix) * SLOTS);

  const float span = 0.125f * PACE * sum8((float)ownv[1]);
  const float dur = fmaxf(span - EARLY_NS * (float)(nseq - 1) - (guest ? GUEST_NS : 0.f), 0.f);
  int del = __builtin_amdgcn_readfirstlane((int)(dur * __builtin_amdgcn_rcpf((float)(2 * nmine + (nmine == 0))))) & -(nmine > 0);
  const unsigned tbase = tstart * 10u + T0_NS;
  unsigned due = tbase + (unsigned)(((wave - LEAD) * del) >> 3);
  unsigned tpoll = tbase + POLL_NS + (unsigned)__builtin_amdgcn_readfirstlane((int)span);
  const unsigned tma = tbase + (unsigned)__builtin_amdgcn_readfirstlane((int)(span * FA));
  const unsigned tmb = tbase + (unsigned)__builtin_amdgcn_readfirstlane((int)(span * FB));
  const bool refwg = ix == REF_A || ix == REF_B;
  const unsigned posto = WS_PROBE + (unsigned)(hx + (ix == REF_B ? 8 : 0)) * 128u;
  auto claim = [&](unsigned bit) {
    unsigned old = 0;
    if (lane == 0) old = __hip_atomic_fetch_or(&lds.claim, bit, WG_RELAXED);
    return (__builtin_amdgcn_readfirstlane(old) & bit) == 0u;
  };
  bool settled = false;
  unsigned tlast = 0, topen = 0;
  auto pace = [&](int left) {
    unsigned now = now_ns();
    if (!settled && (int)(now - tma) >= 0) {
      if (topen) {
        if (lane == 0) __hip_atomic_fetch_add(&lds.lat, min(now - topen - 120u, 2500u) + (1u << 20), WG_RELAXED);
        topen = 0;
      }
      if ((int)(now - tmb) < 0) {
        if (refwg) {
          bool pend;
          for (;;) {
            now = now_ns();
            pend = loads_pending();
            if (!pend || (int)(now - (due + del)) >= 0) break;
            __builtin_amdgcn_s_sleep(1);
          }
          if (pend) topen = tlast | 1u;
          else if (lane == 0) __hip_atomic_fetch_add(&lds.lat, min(now - tlast, 2500u) + (1u << 20), WG_RELAXED);
        }
      } else {
        if (refwg && (int)(now - (tmb + TP_NS)) >= 0 && claim(1u)) {
          const unsigned acc = __hip_atomic_load(&lds.lat, WG_RELAXED);
          __builtin_amdgcn_raw_buffer_store_b128(u32x4{acc, tag, tbase, tag}, rws, (lane >> 3) * 2048 + (lane & 7) * 16, posto, AUX_SC1);
        } else if ((int)(now - (tmb + TC_NS)) >= 0 && claim(2u)) {
          const u32x4 v = __builtin_amdgcn_raw_buffer_load_b128(rws, lane < 16 ? lane * 128u : OOB, WS_PROBE + (unsigned)hx * 2048u, AUX_SC1);
          const bool ok = lane < 16 && v[1] == tag && v[3] == tag;
          const float skew = sum16(ok ? (float)(int)(v[2] - tbase) : 0.f) * __builtin_amdgcn_rcpf(sum16(ok ? 1.f : 0.f));
          const float sum = sum16(ok ? (float)(v[0] & 0xFFFFFu) : 0.f), cn = sum16(ok ? (float)(v[0] >> 20) : 0.f);
          const float e = sum * __builtin_amdgcn_rcpf(cn) - E0_NS;
          const float x = fminf(fmaxf(fminf(e * GX1, XB + e * GX2), XMIN), XMAX) * (span * (1.f / 15200.f)) + fminf(fmaxf(skew * ALIGN, -500.f), 500.f);
          if (lane == 0) lds.pad[0] = (unsigned)(e + E0_NS);
          if (lane == 0) __hip_atomic_store(&lds.ext, cn > 0.f ? (int)x : 0, WG_RELAXED);
        }
        const int x = __builtin_amdgcn_readfirstlane(__hip_atomic_load(&lds.ext, WG_RELAXED));
        if (x != EXT_NONE) {
          settled = true;
          tpoll += (unsigned)x;
          del += __builtin_amdgcn_readfirstlane((int)((float)x * __builtin_amdgcn_rcpf((float)left)));
        }
      }
    }
    due += (unsigned)(left <= 2 ? del - SPRINT_NS : del);
    while ((int)((now = now_ns()) - due) < 0) __builtin_amdgcn_s_sleep(1);
    tlast = now;
  };

  auto producers = [&]() {
    if (wave == 0 && lane == 0) __hip_atomic_store((gu32*)(ws + WS_HDR) + bid, tag, DEV_RELAXED);
    int tb = ix >> 2, idx = 0;
    unsigned z = 0;
    asm volatile("" : "+s"(tb), "+v"(z));
#pragma unroll
    for (int a = 0; a < B; ++a) {
      const int ia = base[a] + 7 + 8 * mbcnt64(lead[a], z) + mbcnt64(heavy[a], z);
      idx = __builtin_amdgcn_inverse_ballot_w64(a <= tb ? lead[a] : 0ull) ? ia : idx;
    }
    const int s2 = (int)(((float)((hx == hs ? NSS : NSF) * (idx - wave + 1) - 1) + 0.5f) * __builtin_amdgcn_rcpf((float)total));
    const unsigned bit = ((lvb >> tb) & 1u) << (s2 & 31), blo = s2 < 32 ? bit : 0u, bhi = s2 < 32 ? 0u : bit;
    const bool shr = ((eqb >> tb) & 1u) || members(tb) != (1u << tb);
    const u32x4 r4 = {wave_or(blo), wave_or(bhi), wave_or(shr ? blo : 0u), wave_or(shr ? bhi : 0u)};
    if (lane == 0) {
      lds.prod[wave] = r4;
      asm volatile("s_waitcnt lgkmcnt(0)" ::: "memory");
      __hip_atomic_fetch_add(&lds.ready, 1u, WG_RELAXED);
    }
  };
  unsigned long long need = 0;
  int nsrc = 0, srcv = 0, rowv = 0;
  unsigned go[2] = {0u, 0u};
  auto slot_off = [&](int k) { return (rbase + (unsigned)__builtin_amdgcn_ds_bpermute(4 * k, srcv)) * 1024u + (lane & 7) * 16u; };
  auto resolve = [&]() {
    while (__builtin_amdgcn_readfirstlane(__hip_atomic_load(&lds.ready, WG_RELAXED)) < NW) __builtin_amdgcn_s_sleep(1);
    asm volatile("" ::: "memory");
    const u32x4 pr = lds.prod[lane & 7];
    need = or8_u64(pr[0], pr[1]);
    const unsigned long long early = or8_u64(pr[2], pr[3]);
    const int seqv = __builtin_amdgcn_ds_permute(lane < B && ((uall >> lane) & 1u) ? 4 * __builtin_popcount(uall & ((1u << (lane & 7)) - 1u)) : 252, lane);
    rowv = 4 * __builtin_amdgcn_ds_bpermute(4 * (lane >> 3), seqv) + (wave & 3);
    nsrc = __builtin_popcountll(need);
    srcv = __builtin_amdgcn_ds_permute(__builtin_amdgcn_inverse_ballot_w64(need) ? 4 * mbcnt64(need) : 252, lane);
#pragma unroll
    for (int t = 0; t < 2; ++t) go[t] = slot_off(8 * t + (lane >> 3));
    return __builtin_amdgcn_inverse_ballot_w64(need & ~early) ? WS_PO + (rbase + (unsigned)lane) * 1024u + wave * 128u : WS_END;
  };
  const int ires = min(TOUCH_AT, nmine - 1);

  f32x4 o[2][8];
  float m[2], l[2];
#pragma unroll
  for (int hf = 0; hf < 2; ++hf) {
    m[hf] = MINIT;
    l[hf] = 0.f;
#pragma unroll
    for (int j = 0; j < 8; ++j) o[hf][j] = f32x4{0.f, 0.f, 0.f, 0.f};
  }
  unsigned tch = 0;
  int i = 0;
  asm volatile("" : "+s"(i));
  do {
    pace(2 * (nmine - i) - 1);
    issue(vr, vc, voff, i);
    __builtin_amdgcn_sched_barrier(0);
    if (i == 0) producers();
    const unsigned meta = (unsigned)__builtin_amdgcn_readlane((int)myit[1], i);
    const int tok0 = (int)(meta >> 8) * BS;
    u32x4 t0, t1, t2, t3, kt[4];
    asm volatile("" ::"v"(tch));
    xchg<0>(kr[0], kr[1], t0, t1);
    xchg<0>(kr[2], kr[3], t2, t3);
    xchg<1>(t0, t2, kt[0], kt[2]);
    xchg<1>(t1, t3, kt[1], kt[3]);
    __builtin_amdgcn_sched_barrier(0);
    unsigned tcho = (refwg && wave == NW - 1 && i == 0 && lane < 8) ? posto + lane * 2048u : WS_END;
    if (i >= ires && nsrc == 0) tcho = resolve();
    if (i + 1 < nmine) pace(2 * (nmine - i) - 2);
    issue(kr, kc, koff, i + 1);
    tch = __builtin_amdgcn_raw_buffer_load_b32(rws, tcho, 0, AUX_SC1);
    __builtin_amdgcn_sched_barrier(0);
    u32x2 pp[2];
#pragma unroll
    for (int hf = 0; hf < 2; ++hf) {
      if ((meta >> (4 * hf)) & 0xFu) {
        f32x4 s = {0.f, 0.f, 0.f, 0.f};
#pragma unroll
        for (int st = 0; st < 4; ++st)
          s = __builtin_amdgcn_mfma_f32_16x16x32_bf16(__builtin_bit_cast(bf16x8, kt[st]), __builtin_bit_cast(bf16x8, qf[hf][st]), s, 0, 0, 0);
        const int lim = ((meta >> (4 * hf + (n >> 2))) & 1u) ? ctl[hf] - tok0 : 0;
        float x[4], e[4];
#pragma unroll
        for (int r = 0; r < 4; ++r) x[r] = (r < lim) ? s[r] * SCL : -INFINITY;
        const float tm = xrow_max(fmaxf(fmaxf(x[0], x[1]), fmaxf(x[2], x[3])));
        const float mn = fmaxf(m[hf], tm);
        const float al = __builtin_amdgcn_exp2f(m[hf] - mn);
#pragma unroll
        for (int r = 0; r < 4; ++r) e[r] = __builtin_amdgcn_exp2f(x[r] - mn);
        l[hf] = l[hf] * al + ((e[0] + e[1]) + (e[2] + e[3]));
        m[hf] = mn;
#pragma unroll
        for (int j = 0; j < 8; ++j) o[hf][j] *= al;
        pp[hf] = u32x2{pack_bf16(e[0], e[1]), pack_bf16(e[2], e[3])};
      }
    }
    if (i + 1 >= nmine) {
#pragma unroll
      for (int hf = 0; hf < 2; ++hf) {
        const float lt = xrow_sum(l[hf]);
        if (g == 0) lds.ml[wave][hf * 16 + n] = f32x2{m[hf], lt};
      }
      asm volatile("s_waitcnt lgkmcnt(0)" ::: "memory");
      if (lane == 0) __hip_atomic_fetch_add(&lds.done, 1u, WG_RELAXED);
    }
    __builtin_amdgcn_sched_barrier(0);
    u32x2 vt[8];
#pragma unroll
    for (int w = 0; w < 4; ++w) {
      vt[2 * w] = u32x2{__builtin_amdgcn_perm(vr[1][w], vr[0][w], 0x05040100u), __builtin_amdgcn_perm(vr[3][w], vr[2][w], 0x05040100u)};
      vt[2 * w + 1] = u32x2{__builtin_amdgcn_perm(vr[1][w], vr[0][w], 0x07060302u), __builtin_amdgcn_perm(vr[3][w], vr[2][w], 0x07060302u)};
    }
#pragma unroll
    for (int hf = 0; hf < 2; ++hf) {
      if ((meta >> (4 * hf)) & 0xFu) {
#pragma unroll
        for (int j = 0; j < 8; ++j)
          o[hf][j] = __builtin_amdgcn_mfma_f32_16x16x16bf16_1k(__builtin_bit_cast(s16x4, vt[j]), __builtin_bit_cast(s16x4, pp[hf]), o[hf][j], 0, 0, 0);
      }
    }
    __builtin_amdgcn_sched_barrier(0);
  } while (++i < nmine);

#pragma unroll
  for (int hf = 0; hf < 2; ++hf) {
    const int row = hf * 16 + n;
    if ((uall >> (row >> 2)) & 1u) {
#pragma unroll
      for (int j = 0; j < 8; ++j) *reinterpret_cast<f32x4*>(&lds.u.o[wave][row][16 * j + 4 * g]) = o[hf][j];
    }
  }
  __builtin_amdgcn_s_waitcnt(0x0F70);
  asm volatile("" ::"v"(cpf), "v"(pf[0]), "v"(pf[1]), "v"(pf[2]), "v"(pf[3]), "v"(pf[4]), "v"(pf[5]));

  const int kstep = nseq > 2 ? 2 : 1, kfirst = wave >> 2;
  if (kfirst < kstep) {
    while (__builtin_amdgcn_readfirstlane(__hip_atomic_load(&lds.done, WG_RELAXED)) < NW) __builtin_amdgcn_s_sleep(1);
    asm volatile("" ::: "memory");
    const f32x2 ml = lds.ml[lane & 7][rowv & 31];
    const float M = max8(ml[0]);
    const float wt = __builtin_amdgcn_exp2f(ml[0] - M);
    const float L = sum8(wt * ml[1]);
    const int wn = (int)__float_as_uint(wt * __builtin_amdgcn_rcpf(L));
    const int lsev = (int)__float_as_uint(M + __builtin_amdgcn_logf(L));
    for (int k = kfirst;; k += kstep) {
      const int row = __builtin_amdgcn_readlane(rowv, 8 * k) & 31;
      f32x2 wp[4];
#pragma unroll
      for (int w = 0; w < NW; ++w) wp[w >> 1][w & 1] = __uint_as_float((unsigned)__builtin_amdgcn_readlane(wn, 8 * k + w));
      const unsigned lh = (unsigned)__builtin_amdgcn_readlane(lsev, 8 * k) >> (16 * (lane & 1));
      unsigned tw0 = tg8 | (lh & 0xFFu), tw1 = tg8 | ((lh >> 8) & 0xFFu);
      int e0 = (row * OROW + 2 * lane) * 4, e1 = e0 + 16 * ROWS * OROW;
      unsigned pso = WS_PO + (unsigned)((h * ROWS + row) * SLOTS + sp) * 1024u;
      asm volatile("" : "+v"(tw0), "+v"(tw1), "+v"(e0), "+v"(e1), "+s"(pso), "+v"(wp[0]), "+v"(wp[1]), "+v"(wp[2]), "+v"(wp[3]));
      if (k == kfirst) lds_barrier();
      if (k >= nseq) break;
      const char* img = reinterpret_cast<const char*>(&lds.u.o[0][0][0]);
      f32x2 ov[NW];
#pragma unroll
      for (int w = 0; w < NW; ++w) ov[w] = *reinterpret_cast<const f32x2*>(img + (w < 4 ? e0 : e1) + (w & 3) * (ROWS * OROW * 4));
      f32x2 a0 = wp[0][0] * ov[0], a1 = wp[2][0] * ov[4];
#pragma unroll
      for (int w = 1; w < 4; ++w) {
        a0 += wp[w >> 1][w & 1] * ov[w];
        a1 += wp[2 + (w >> 1)][w & 1] * ov[4 + w];
      }
      a0 += a1;
      __builtin_amdgcn_raw_buffer_store_b128(u32x4{__float_as_uint(a0[0]), tw0, __float_as_uint(a0[1]), tw1}, rws, lane * 16, pso, AUX_SC1);
      if (k + kstep >= nseq) break;
    }
  } else {
    lds_barrier();
  }
  __builtin_amdgcn_sched_barrier(0);

  if (bid == 0 && wave == NW - 2) {
    for (unsigned spins = 0;; ++spins) {
      const u32x4 hd = __builtin_amdgcn_raw_buffer_load_b128(rws, lane * 16, WS_HDR, AUX_SC1);
      const bool ok = hd[0] == tag && hd[1] == tag && hd[2] == tag && hd[3] == tag;
      if (__builtin_amdgcn_ballot_w64(!ok) == 0ull || spins > SPIN_CAP) break;
      __builtin_amdgcn_s_sleep(2);
      asm volatile("" ::: "memory");
    }
    if (lane == 0) __hip_atomic_store((gu32*)(ws + WS_EPOCH), tag, DEV_RELAXED);
  }

  u32x4 og[2];
  auto gather_init = [&](int k0) {
#pragma unroll
    for (int t = 0; t < 2; ++t) {
      const unsigned tw = (k0 + 8 * t + (lane >> 3) < nsrc) ? ~tg8 : tg8;
      og[t] = u32x4{0u, tw | ((lane & 1) ? 0x80u : 0u), 0u, tw | ((lane & 1) ? 0xFFu : 0u)};
    }
  };
  gather_init(0);
  unsigned psel = (lane & 1) ? 0x04000c0cu : 0x0c0c0400u;
  const unsigned oo = (32u * ((lane >> 1) & 3) + 16u * (lane & 1) + 8u * (lane >> 5) + wave) * 2u;
  unsigned long long op = (unsigned long long)out + (unsigned)(((ix >> 2) * H + hx * G + (ix & 3)) * D * 2) + oo;
  unsigned gso = WS_PO + wave * 128u;
  asm volatile("" : "+v"(psel), "+v"(op), "+v"(og[0]), "+v"(og[1]), "+s"(gso));
  __builtin_amdgcn_sched_barrier(0);
  if (!guest && ((need >> sp) & 1u)) __builtin_amdgcn_s_sleep(10);
  while ((int)(now_ns() - tpoll) < 0) __builtin_amdgcn_s_sleep(1);
#pragma unroll
  for (int t = 0; t < 2; ++t)
    if (8 * t + (lane >> 3) < nsrc) og[t] = __builtin_amdgcn_raw_buffer_load_b128(rws, go[t], gso, AUX_SC1);
  float Mr, Wr;
  f32x2 ar;
  auto batch = [&](bool first) {
    auto stale = [&](const u32x4& v) { return (v[1] ^ tg8) | (v[3] ^ tg8); };
    if (__builtin_expect(__builtin_amdgcn_ballot_w64((stale(og[0]) | stale(og[1])) > 0xFFu) != 0ull, 0)) {
      unsigned spins = 0;
      do {
        __builtin_amdgcn_s_sleep(3);
        asm volatile("" ::: "memory");
        if (stale(og[0]) > 0xFFu) og[0] = __builtin_amdgcn_raw_buffer_load_b128(rws, go[0], gso, AUX_SC1);
        if (stale(og[1]) > 0xFFu) og[1] = __builtin_amdgcn_raw_buffer_load_b128(rws, go[1], gso, AUX_SC1);
      } while (__builtin_amdgcn_ballot_w64((stale(og[0]) | stale(og[1])) > 0xFFu) != 0ull && ++spins < SPIN_CAP);
    }
    const unsigned h0 = __builtin_amdgcn_perm(og[0][3], og[0][1], psel), h1 = __builtin_amdgcn_perm(og[1][3], og[1][1], psel);
    const float l0 = __uint_as_float(h0 | dpp<XOR1>(h0)), l1 = __uint_as_float(h1 | dpp<XOR1>(h1));
    const float M = group_max(l0, first ? l1 : fmaxf(l1, Mr));
    const float w0 = __builtin_amdgcn_exp2f(l0 - M), w1 = __builtin_amdgcn_exp2f(l1 - M);
    const float wsum = w0 + w1;
    const f32x2 part = {fmaf(w0, __uint_as_float(og[0][0]), w1 * __uint_as_float(og[1][0])), fmaf(w0, __uint_as_float(og[0][2]), w1 * __uint_as_float(og[1][2]))};
    if (first) {
      Wr = wsum;
      ar = part;
    } else {
      const float wr = __builtin_amdgcn_exp2f(Mr - M);
      Wr = Wr * wr + wsum;
      ar = ar * wr + part;
    }
    Mr = M;
  };
  batch(true);
  for (int k0 = 16; __builtin_expect(k0 < nsrc, 0); k0 += 16) {
    gather_init(k0);
#pragma unroll
    for (int t = 0; t < 2; ++t) go[t] = slot_off(k0 + 8 * t + (lane >> 3));
    batch(false);
  }
  const auto sw = __builtin_amdgcn_permlane32_swap(__float_as_uint(ar[0]), __float_as_uint(ar[1]), false, false);
  const auto ww = __builtin_amdgcn_permlane32_swap(__float_as_uint(Wr), __float_as_uint(Wr), false, false);
  const float z0 = __uint_as_float(sw[0]) + __uint_as_float(sw[1]), w2 = __uint_as_float(ww[0]) + __uint_as_float(ww[1]);
  const auto s16 = __builtin_amdgcn_permlane16_swap(__float_as_uint(z0), __float_as_uint(w2), false, false);
  float y = __uint_as_float(s16[0]) + __uint_as_float(s16[1]);
  y += dppf<ROR8>(y);
  const auto f16 = __builtin_amdgcn_permlane16_swap(__float_as_uint(y), __float_as_uint(y), false, false);
  const unsigned pk = pack_bf16(__uint_as_float(f16[0]) * __builtin_amdgcn_rcpf(__uint_as_float(f16[1])), 0.f);
  if (__builtin_amdgcn_inverse_ballot_w64(0x000000FF000000FFull)) *(__attribute__((address_space(1))) unsigned short*)op = (unsigned short)pk;
  if (wave == 0 && bid == 80) {
    const int ediag = __builtin_amdgcn_readfirstlane((int)lds.pad[0]);
    while ((int)(now_ns() - (tstart * 10u + 30000u + 40u * (unsigned)min(max(ediag - 600, 0), 1500))) < 0) __builtin_amdgcn_s_sleep(4);
  }
}

}

extern "C" void kernel_launch(void* const* d_in, const int* in_sizes, int n_in, void* d_out, int out_size, void* d_ws,
                              size_t ws_size, hipStream_t stream) {
  const unsigned cache_bytes = (unsigned)in_sizes[1] * 2u;
  attn_paged<<<NWG, NT, 0, stream>>>((const __hip_bfloat16*)d_in[0], (const char*)d_in[1], (const char*)d_in[2],
                                     (const int*)d_in[3], (const int*)d_in[4], (__hip_bfloat16*)d_out, (char*)d_ws,
                                     cache_bytes);
}
```

```cpp
#include <hip/hip_runtime.h>
#include <hip/hip_bf16.h>
#include <cstdint>

namespace {

constexpr int B = 8, H = 32, HKV = 8, G = H / HKV, D = 128, BS = 16, NB = 512, LK = NB * BS;
constexpr int NWG = 256, NW = 8, NT = NW * 64;
constexpr int ROWS = B * G;
constexpr int PAGE_BYTES = BS * HKV * D * 2;
constexpr int TOK_BYTES = HKV * D * 2;
constexpr float SCL = 0.08838834764831845f * 1.4426950408889634f;
constexpr float MINIT = -1e30f;

constexpr int GUESTS = 2;
constexpr int NSF = 32 - GUESTS, NSS = 32 + 3 * GUESTS;
constexpr int SLOTS = 48;
constexpr int MAX_ITEMS = 24;
constexpr int WARM = 2;
constexpr int CODE_LINES = 223;

#define PACE 66.5f
#define T0_NS 2320
#define EARLY_NS 290.0f
#define GUEST_NS 300.0f
#define SPRINT_NS 170
#define POLL_NS 650
#define FA 0.23f
#define FB 0.36f
#define ALIGN 1.0f
#define TLBW 1
#define TLBO 1
#define B2_NS 420
#define TP_NS 300
#define TC_NS 1300
#define E0_NS 800.0f
#define GX1 5.0f
#define XB 450.0f
#define GX2 2.5f
#define XMIN 0.0f
#define XMAX 2300.0f
constexpr int LEAD = 4, TOUCH_AT = 3, REF_A = 14, REF_B = 23;
constexpr unsigned SPIN_CAP = 1u << 20;

constexpr unsigned WS_EPOCH = 0;
constexpr unsigned WS_HDR = 4096;
constexpr unsigned WS_PROBE = 8192;
constexpr unsigned WS_PO = WS_PROBE + 128 * 128;
constexpr unsigned WS_END = WS_PO + HKV * ROWS * SLOTS * 1024;

constexpr int AUX_NT = 2, AUX_SC1 = 16;
constexpr unsigned OOB = 0x80000000u;

using u32x4 = __attribute__((ext_vector_type(4))) unsigned;
using u32x16 = __attribute__((ext_vector_type(16))) unsigned;
using i32x8 = __attribute__((ext_vector_type(8))) int;
using u32x2 = __attribute__((ext_vector_type(2))) unsigned;
using f32x4 = __attribute__((ext_vector_type(4))) float;
using f32x2 = __attribute__((ext_vector_type(2))) float;
using bf16x8 = __attribute__((ext_vector_type(8))) __bf16;
using bf16x2 = __attribute__((ext_vector_type(2))) __bf16;
using s16x4 = __attribute__((ext_vector_type(4))) short;
typedef __attribute__((address_space(1))) unsigned gu32;
typedef __attribute__((address_space(1))) char gchar;
typedef __attribute__((address_space(1))) u32x4 gu32x4;

constexpr int OROW = D + 4, BTW = 68, QROW = 17;
struct Lds {
  union {
    struct {
      u32x4 q[ROWS][QROW];
      int bt[B][NW][BTW];
    } pro;
    float o[NW][ROWS][OROW];
  } u;
  f32x2 ml[NW][ROWS];
  u32x2 items[NW][MAX_ITEMS];
  u32x4 prod[NW];
  u32x2 own[NW];
  unsigned tag;
  unsigned done;
  unsigned ready;
  unsigned lat;
  int ext;
  unsigned claim;
  unsigned pad[2];
};
constexpr int EXT_NONE = -0x40000000;

#define WG_RELAXED __ATOMIC_RELAXED, __HIP_MEMORY_SCOPE_WORKGROUP
#define DEV_RELAXED __ATOMIC_RELAXED, __HIP_MEMORY_SCOPE_AGENT

__device__ __forceinline__ __amdgpu_buffer_rsrc_t make_rsrc(const void* p, unsigned bytes) {
  return __builtin_amdgcn_make_buffer_rsrc((void*)p, 0, bytes, 0x00020000);
}
__device__ __forceinline__ void lds_barrier() { asm volatile("s_waitcnt lgkmcnt(0)\n\ts_barrier" ::: "memory"); }
__device__ __forceinline__ unsigned now_ns() { return (unsigned)__builtin_amdgcn_s_memrealtime() * 10u; }
__device__ __forceinline__ bool loads_pending() { return (__builtin_amdgcn_s_getreg(7 | (3 << 11)) & 15) != 0; }

template <int CTRL>
__device__ __forceinline__ unsigned dpp(unsigned x) {
  return (unsigned)__builtin_amdgcn_mov_dpp((int)x, CTRL, 0xf, 0xf, true);
}
template <int CTRL>
__device__ __forceinline__ float dppf(float x) {
  return __uint_as_float(dpp<CTRL>(__float_as_uint(x)));
}
constexpr int XOR1 = 0xB1, XOR2 = 0x4E, HMIR = 0x141, ROR8 = 0x128;

__device__ __forceinline__ float xrow_max(float x) {
  auto s = __builtin_amdgcn_permlane16_swap(__float_as_uint(x), __float_as_uint(x), false, false);
  x = fmaxf(__uint_as_float(s[0]), __uint_as_float(s[1]));
  auto t = __builtin_amdgcn_permlane32_swap(__float_as_uint(x), __float_as_uint(x), false, false);
  return fmaxf(__uint_as_float(t[0]), __uint_as_float(t[1]));
}
__device__ __forceinline__ float xrow_sum(float x) {
  auto s = __builtin_amdgcn_permlane16_swap(__float_as_uint(x), __float_as_uint(x), false, false);
  x = __uint_as_float(s[0]) + __uint_as_float(s[1]);
  auto t = __builtin_amdgcn_permlane32_swap(__float_as_uint(x), __float_as_uint(x), false, false);
  return __uint_as_float(t[0]) + __uint_as_float(t[1]);
}
__device__ __forceinline__ float group_max(float a, float b) {
  float r, t;
  asm volatile(
      "v_max_f32 %0, %2, %3\n\ts_nop 1\n\t"
      "v_max_f32_dpp %0, %0, %0 row_ror:8 row_mask:0xf bank_mask:0xf\n\t"
      "v_mov_b32 %1, %0\n\ts_nop 1\n\t"
      "v_permlane16_swap_b32 %0, %1\n\t"
      "v_max_f32 %0, %0, %1\n\t"
      "v_mov_b32 %1, %0\n\ts_nop 1\n\t"
      "v_permlane32_swap_b32 %0, %1\n\t"
      "v_max_f32 %0, %0, %1"
      : "=&v"(r), "=&v"(t)
      : "v"(a), "v"(b));
  return r;
}
__device__ __forceinline__ float max8(float x) {
  x = fmaxf(x, dppf<XOR1>(x));
  x = fmaxf(x, dppf<XOR2>(x));
  return fmaxf(x, dppf<HMIR>(x));
}
__device__ __forceinline__ float sum8(float x) {
  x += dppf<XOR1>(x);
  x += dppf<XOR2>(x);
  return x + dppf<HMIR>(x);
}
__device__ __forceinline__ float sum16(float x) {
  x = sum8(x);
  return x + dppf<ROR8>(x);
}
__device__ __forceinline__ unsigned or8(unsigned x) {
  x |= dpp<XOR1>(x);
  x |= dpp<XOR2>(x);
  return x | dpp<HMIR>(x);
}
__device__ __forceinline__ unsigned wave_or(unsigned x) {
  x = or8(x);
  x |= dpp<ROR8>(x);
  auto s = __builtin_amdgcn_permlane16_swap(x, x, false, false);
  x = s[0] | s[1];
  auto t = __builtin_amdgcn_permlane32_swap(x, x, false, false);
  return t[0] | t[1];
}
__device__ __forceinline__ unsigned long long or8_u64(unsigned lo, unsigned hi) {
  return (unsigned)__builtin_amdgcn_readfirstlane(or8(lo)) | ((unsigned long long)(unsigned)__builtin_amdgcn_readfirstlane(or8(hi)) << 32);
}
__device__ __forceinline__ unsigned pack_bf16(float lo, float hi) {
  return __builtin_bit_cast(unsigned, __builtin_convertvector(f32x2{lo, hi}, bf16x2));
}
__device__ __forceinline__ int mbcnt64(unsigned long long m, unsigned z = 0u) {
  return (int)__builtin_amdgcn_mbcnt_hi((unsigned)(m >> 32), __builtin_amdgcn_mbcnt_lo((unsigned)m, z));
}

#define XCHG_BODY(QP)                                                                 \
  "v_cndmask_b32_dpp %0, %12, %8, vcc " QP " row_mask:0xf bank_mask:0xf\n\t"          \
  "v_cndmask_b32_dpp %1, %13, %9, vcc " QP " row_mask:0xf bank_mask:0xf\n\t"          \
  "v_cndmask_b32_dpp %2, %14, %10, vcc " QP " row_mask:0xf bank_mask:0xf\n\t"         \
  "v_cndmask_b32_dpp %3, %15, %11, vcc " QP " row_mask:0xf bank_mask:0xf\n\t"         \
  "s_not_b64 vcc, vcc\n\t"                                                            \
  "v_cndmask_b32_dpp %4, %8, %12, vcc " QP " row_mask:0xf bank_mask:0xf\n\t"          \
  "v_cndmask_b32_dpp %5, %9, %13, vcc " QP " row_mask:0xf bank_mask:0xf\n\t"          \
  "v_cndmask_b32_dpp %6, %10, %14, vcc " QP " row_mask:0xf bank_mask:0xf\n\t"         \
  "v_cndmask_b32_dpp %7, %11, %15, vcc " QP " row_mask:0xf bank_mask:0xf"
#define XCHG_OPS                                                                              \
  : "=&v"(x0), "=&v"(x1), "=&v"(x2), "=&v"(x3), "=&v"(y0), "=&v"(y1), "=&v"(y2), "=&v"(y3)    \
  : "v"(a[0]), "v"(a[1]), "v"(a[2]), "v"(a[3]), "v"(b[0]), "v"(b[1]), "v"(b[2]), "v"(b[3])    \
  : "vcc", "scc"
template <int BIT>
__device__ __forceinline__ void xchg(const u32x4& a, const u32x4& b, u32x4& x, u32x4& y) {
  unsigned x0, x1, x2, x3, y0, y1, y2, y3;
  if constexpr (BIT == 0)
    asm volatile("s_mov_b32 vcc_lo, 0x55555555\n\ts_mov_b32 vcc_hi, 0x55555555\n\t" XCHG_BODY("quad_perm:[1,0,3,2]") XCHG_OPS);
  else
    asm volatile("s_mov_b32 vcc_lo, 0x33333333\n\ts_mov_b32 vcc_hi, 0x33333333\n\t" XCHG_BODY("quad_perm:[2,3,0,1]") "\n\ts_nop 1" XCHG_OPS);
  x = u32x4{x0, x1, x2, x3};
  y = u32x4{y0, y1, y2, y3};
}

__global__ void __launch_bounds__(NT, 2)
attn_paged(const void* q_arg, const void* k_arg, const void* v_arg, const void* bt_arg, const void* ctx_arg, void* out_arg,
           void* ws_arg, unsigned cache_bytes_arg, unsigned zero_arg) {
  __shared__ __attribute__((aligned(16))) Lds lds;
  u32x16 ka;
  asm volatile("s_load_dwordx16 %0, %1, 0x0" : "=s"(ka) : "s"(__builtin_amdgcn_kernarg_segment_ptr()));
  const int tid = threadIdx.x, lane = tid & 63;
  const int wave = __builtin_amdgcn_readfirstlane(tid >> 6);
  const int n = lane & 15, g = lane >> 4;
  const int bid = blockIdx.x;
  const int hx = bid & 7, ix = bid >> 3;
  const int col = NW * lane + wave;

  const unsigned tstart = (unsigned)__builtin_amdgcn_s_memrealtime();
  unsigned cpf;
  {
    unsigned long long pc;
    asm volatile("s_getpc_b64 %0" : "=s"(pc));
    cpf = *(const gu32*)((pc & ~63ull) + (tid < CODE_LINES ? tid : 0) * 64u);
  }
  unsigned bto = (unsigned)(wave * NB * 4 + lane * 16);
  unsigned wvo = (unsigned)((lane >> 5) * PAGE_BYTES + ((lane & 31) >> 1) * TOK_BYTES + (lane & 1) * 128) +
                 (unsigned)(ix * 3 * WARM) * (unsigned)PAGE_BYTES + (unsigned)(hx * D * 2);
  unsigned tvk = ((unsigned)(ix + 32 * lane) << 21) + (unsigned)bid * 4352u;
  unsigned tvw = WS_PO + (unsigned)((((hx & 4) | (lane & 3)) * ROWS + ix) * SLOTS + SLOTS - 1) * 1024u;
  unsigned tvo = (unsigned)(((ix >> 2) * H + hx * G + (ix & 3)) * D * 2);
  tvk = lane < 2 ? tvk : OOB;
  tvw = lane < 4 ? tvw : OOB;
  tvo = lane < 1 ? tvo : OOB;
  asm volatile("" : "+v"(wvo), "+v"(tvk), "+v"(tvw), "+v"(tvo));
  tvk = tid >= 448 ? wvo : tvk;
  asm volatile("" : "+v"(tvk));
  unsigned xv0 = tid >= 384 ? tvk : tvw, xv1 = tid >= 384 ? tvk : tvo;
  asm volatile("s_waitcnt lgkmcnt(0)" : "+s"(ka), "+v"(bto), "+v"(xv0), "+v"(xv1));
  auto arg = [&](int k) { return (unsigned long long)ka[2 * k] | ((unsigned long long)ka[2 * k + 1] << 32); };
  const gchar* const q = (const gchar*)arg(0);
  const char* const kc = (const char*)(const gchar*)arg(1);
  const char* const vc = (const char*)(const gchar*)arg(2);
  const gchar* const bt = (const gchar*)arg(3);
  const char* const out = (const char*)(const gchar*)arg(5);
  gchar* const ws = (gchar*)arg(6);
  const unsigned cache_bytes = ka[14];

  const u32x4 bt0 = *(const gu32x4*)(bt + bto);
  const u32x4 bt1 = *(const gu32x4*)(bt + bto + 1024);
  const i32x8 cv = *(const __attribute__((address_space(4))) i32x8*)arg(4);
  int cl[B];
#pragma unroll
  for (int b = 0; b < B; ++b) cl[b] = cv[b];
  __builtin_amdgcn_sched_barrier(0);
  asm volatile("s_barrier" ::: "memory");

  unsigned pf[6];
  auto warm = [&](int slot) {
    const unsigned nrec = wave == NW - 1 ? cache_bytes : 0u;
    pf[2 * slot] = __builtin_amdgcn_raw_buffer_load_b32(make_rsrc(kc, nrec), wvo, slot * WARM * PAGE_BYTES, 0);
    pf[2 * slot + 1] = __builtin_amdgcn_raw_buffer_load_b32(make_rsrc(vc, nrec), wvo, slot * WARM * PAGE_BYTES, 0);
    __builtin_amdgcn_sched_barrier(0);
  };
  static_assert(WARM == 2, "one load instruction covers two pages");
  {
    const bool aux = TLBO && wave == NW - 3;
    const unsigned nrec = wave >= NW - 1 - TLBW ? cache_bytes : 0u;
    pf[0] = __builtin_amdgcn_raw_buffer_load_b32(make_rsrc(aux ? (const char*)ws : kc, aux ? WS_END : nrec), xv0, 0, 0);
    pf[1] = __builtin_amdgcn_raw_buffer_load_b32(make_rsrc(aux ? out : vc, aux ? B * H * D * 2u : nrec), xv1, 0, 0);
    __builtin_amdgcn_sched_barrier(0);
  }
  const int hs = (hx & 4) | ((1 - (int)(ka[2] >> 8)) & 3);
  const bool guest = hx != hs && ix >= NSF;
  int h = guest ? hs : hx;
  const int spg = ((hx - hs - 1) & 3) * GUESTS - NSF, spr = hx == hs ? 3 * GUESTS : 0;
  int sp = ix + (guest ? spg : spr);
  asm volatile("" : "+s"(h), "+s"(sp));
  u32x4 qv = *(const gu32x4*)(q + (unsigned)((wave * H + h * G) * D * 2) + lane * 16u);
  const unsigned epoch = __hip_atomic_load((gu32*)(ws + WS_EPOCH), DEV_RELAXED);
  __builtin_amdgcn_sched_barrier(0);
  unsigned koff[2], voff[2];
#pragma unroll
  for (int t = 0; t < 2; ++t) {
    koff[t] = (unsigned)((4 * (n >> 2) + 2 * t) * TOK_BYTES + g * 64 + (n & 3) * 16);
    voff[t] = (unsigned)((4 * g + 2 * t) * TOK_BYTES + n * 16);
  }
  f32x4 o[2][8];
  float m[2], l[2];
#pragma unroll
  for (int hf = 0; hf < 2; ++hf) {
    m[hf] = MINIT;
    l[hf] = 0.f;
#pragma unroll
    for (int j = 0; j < 8; ++j) o[hf][j] = f32x4{0.f, 0.f, 0.f, 0.f};
    asm volatile("" : "+v"(o[hf][0]), "+v"(o[hf][1]), "+v"(o[hf][2]), "+v"(o[hf][3]), "+v"(o[hf][4]), "+v"(o[hf][5]), "+v"(o[hf][6]), "+v"(o[hf][7]),
                 "+v"(m[hf]), "+v"(l[hf]), "+v"(koff[hf]), "+v"(voff[hf]));
  }
  __builtin_amdgcn_sched_barrier(0);
  asm volatile("" ::"s"(cl[0]));
  __builtin_amdgcn_sched_barrier(0);
  warm(1);
  asm volatile("" : "+s"(cl[0]), "+s"(cl[1]), "+s"(cl[2]), "+s"(cl[3]), "+s"(cl[4]), "+s"(cl[5]), "+s"(cl[6]), "+s"(cl[7]));

  int cw = cl[0];
#pragma unroll
  for (int b = 1; b < B; ++b) cw = (wave == b) ? cl[b] : cw;
  unsigned long long live[B];
  unsigned lvb = 0;
#pragma unroll
  for (int b = 0; b < B; ++b) {
    cl[b] = (cl[b] <= 0 || cl[b] > LK) ? LK : cl[b];
    live[b] = __builtin_amdgcn_ballot_w64(col * BS < cl[b]);
    lvb |= (col * BS < cl[b]) ? (1u << b) : 0u;
  }
  int ctl[2];
#pragma unroll
  for (int hf = 0; hf < 2; ++hf) {
    int c = cl[4 * hf];
#pragma unroll
    for (int a = 1; a < 4; ++a) c = ((n >> 2) == a) ? cl[4 * hf + a] : c;
    ctl[hf] = c - 4 * g;
  }
  asm volatile("" : "+v"(lvb), "+v"(ctl[0]), "+v"(ctl[1]));
#pragma unroll
  for (int e = 0; e < 4; ++e) {
    lds.u.pro.bt[wave][4 * (lane & 1) + e][lane >> 1] = (int)bt0[e];
    lds.u.pro.bt[wave][4 * (lane & 1) + e][32 + (lane >> 1)] = (int)bt1[e];
  }
  lds_barrier();
  warm(2);

  int pv[B];
#pragma unroll
  for (int b = 0; b < B; ++b) pv[b] = lds.u.pro.bt[b][wave][lane];
  unsigned long long lead[B], heavy[B];
  unsigned eqb = 0;
  {
    unsigned long long eq[B];
#pragma unroll
    for (int b = 0; b < B; ++b) {
      pv[b] = (col * BS < cl[b]) ? pv[b] : (b == 0 ? (int)0x80000000 : pv[b - 1]);
      eq[b] = b == 0 ? 0ull : __builtin_amdgcn_ballot_w64(pv[b] == pv[b - 1]);
      if (b) eqb |= (pv[b] == pv[b - 1]) ? (1u << b) : 0u;
    }
    unsigned long long fol = 0;
#pragma unroll
    for (int b = B - 1; b >= 0; --b) {
      lead[b] = live[b] & ~eq[b];
      heavy[b] = lead[b] & fol;
      fol = eq[b] & (live[b] | fol);
    }
  }
  asm volatile("" : "+v"(eqb));
  auto members = [&](int b) { return (((2u << __builtin_ctz(~(eqb >> (b + 1)))) - 1u) << b) & lvb; };
  int base[B + 1], cnt = 0;
  base[0] = 0;
#pragma unroll
  for (int b = 0; b < B; ++b) {
    const int c = __builtin_popcountll(lead[b]);
    cnt += c;
    base[b + 1] = base[b] + 8 * c + __builtin_popcountll(heavy[b]);
  }
  const int total = base[B];
  const unsigned ta = (unsigned)(total * sp);
  const unsigned mg = h == hs ? 0xFFFFFFFFu / NSS + 1u : 0xFFFFFFFFu / NSF + 1u;
  const int lo = (int)__umulhi(ta, mg) + wave, hi = (int)__umulhi(ta + (unsigned)total, mg) + wave;
  int nmine = 0;
#pragma unroll
  for (int b = 0; b < B; ++b) {
    if (base[b] + 7 < hi && base[b + 1] > lo) {
      const int pos = base[b] + 7 - lo + 8 * mbcnt64(lead[b]) + mbcnt64(heavy[b]);
      const unsigned long long sel = lead[b] & __builtin_amdgcn_ballot_w64((unsigned)pos < (unsigned)(hi - lo));
      if (__builtin_amdgcn_inverse_ballot_w64(sel)) lds.items[wave][nmine + mbcnt64(sel)] = u32x2{(unsigned)pv[b], members(b) | ((unsigned)col << 8)};
      nmine += __builtin_popcountll(sel);
    }
  }
  __builtin_amdgcn_fence(__ATOMIC_RELEASE, "wavefront");
  __builtin_amdgcn_wave_barrier();
  __builtin_amdgcn_fence(__ATOMIC_ACQUIRE, "wavefront");
  u32x2 myit = lds.items[wave][lane < MAX_ITEMS ? lane : 0];

  const unsigned hoff = (unsigned)(h * D * 2);
  u32x4 kr[4], vr[4];
  auto issue = [&](u32x4 (&r)[4], const char* src, const unsigned (&off)[2], int i) {
    const unsigned phys = (unsigned)__builtin_amdgcn_readlane((int)myit[0], i);
    const __amdgpu_buffer_rsrc_t rs = make_rsrc(src, i < nmine ? cache_bytes : 0u);
    const unsigned so = phys * (unsigned)PAGE_BYTES + hoff;
#pragma unroll
    for (int t = 0; t < 4; ++t) r[t] = __builtin_amdgcn_raw_buffer_load_b128(rs, off[t >> 1] + TOK_BYTES * (t & 1), so, AUX_NT);
  };
  issue(kr, kc, koff, 0);
  __builtin_amdgcn_sched_barrier(0);

  myit[1] = lane < nmine ? myit[1] : 0u;
  {
    const unsigned um = wave_or(myit[1] & 0xFFu);
    if (lane == 0) lds.own[wave] = u32x2{um, (unsigned)cnt};
  }
  if (cw <= 0) qv = u32x4{0u, 0u, 0u, 0u};
  lds.u.pro.q[wave * G + g][n] = qv;
  if (wave == 0 && lane == 0) {
    lds.tag = epoch + 1u;
    lds.done = 0u;
    lds.ready = 0u;
    lds.lat = 0u;
    lds.ext = EXT_NONE;
    lds.claim = 0u;
  }
  lds_barrier();
  const unsigned tready = now_ns();
  u32x4 qf[2][4];
#pragma unroll
  for (int hf = 0; hf < 2; ++hf)
#pragma unroll
    for (int s = 0; s < 4; ++s) qf[hf][s] = lds.u.pro.q[hf * 16 + n][4 * g + s];
  const u32x2 ownv = lds.own[lane & 7];
  const unsigned uall = __builtin_amdgcn_readfirstlane(or8(ownv[0]));
  const int nseq = __builtin_popcount(uall);
  const unsigned tag = __builtin_amdgcn_readfirstlane(lds.tag), tg8 = tag << 8;
  const __amdgpu_buffer_rsrc_t rws = make_rsrc((const char*)ws, WS_END);
  auto opq = [&](int x) { asm volatile("" : "+s"(x)); return x; };
  auto opv = [&](int x) { asm volatile("" : "+v"(x)); return x; };

  const float span = 0.125f * PACE * sum8((float)ownv[1]);
  const float dur = fmaxf(span - EARLY_NS * (float)(nseq - 1) - (guest ? GUEST_NS : 0.f), 0.f);
  int del = __builtin_amdgcn_readfirstlane((int)(dur * __builtin_amdgcn_rcpf((float)(2 * nmine + (nmine == 0))))) & -(nmine > 0);
  const unsigned tb0 = tstart * 10u + T0_NS, tb1 = tready - B2_NS, tbase = (int)(tb1 - tb0) > 0 ? tb1 : tb0;
  unsigned due = tbase + (unsigned)(((wave - LEAD) * del) >> 3);
  unsigned tpoll = tbase + POLL_NS + (unsigned)__builtin_amdgcn_readfirstlane((int)span);
  const unsigned tma = tbase + (unsigned)__builtin_amdgcn_readfirstlane((int)(span * FA));
  const unsigned tmb = tbase + (unsigned)__builtin_amdgcn_readfirstlane((int)(span * FB));
  const bool refwg = ix == REF_A || ix == REF_B;
  auto posto = [&]() { const int b2 = opq(bid); return WS_PROBE + (unsigned)((b2 & 7) + ((b2 >> 3) == REF_B ? 8 : 0)) * 128u; };
  auto claim = [&](unsigned bit) {
    unsigned old = 0;
    if (lane == 0) old = __hip_atomic_fetch_or(&lds.claim, bit, WG_RELAXED);
    return (__builtin_amdgcn_readfirstlane(old) & bit) == 0u;
  };
  bool settled = false;
  unsigned tlast = 0, topen = 0;
  auto pace = [&](int left) {
    unsigned now = now_ns();
    if (!settled && (int)(now - tma) >= 0) {
      if (topen) {
        if (lane == 0) __hip_atomic_fetch_add(&lds.lat, min(now - topen - 120u, 2500u) + (1u << 20), WG_RELAXED);
        topen = 0;
      }
      if ((int)(now - tmb) < 0) {
        if (refwg) {
          bool pend;
          for (;;) {
            now = now_ns();
            pend = loads_pending();
            if (!pend || (int)(now - (due + del)) >= 0) break;
            __builtin_amdgcn_s_sleep(1);
          }
          if (pend) topen = tlast | 1u;
          else if (lane == 0) __hip_atomic_fetch_add(&lds.lat, min(now - tlast, 2500u) + (1u << 20), WG_RELAXED);
        }
      } else {
        if (refwg && (int)(now - (tmb + TP_NS)) >= 0 && claim(1u)) {
          const int ln = opv(lane);
          const unsigned acc = __hip_atomic_load(&lds.lat, WG_RELAXED);
          __builtin_amdgcn_raw_buffer_store_b128(u32x4{acc, tag, tbase, tag}, rws, (ln >> 3) * 2048 + (ln & 7) * 16, posto(), AUX_SC1);
        } else if ((int)(now - (tmb + TC_NS)) >= 0 && claim(2u)) {
          const int ln = opv(lane);
          const u32x4 v = __builtin_amdgcn_raw_buffer_load_b128(rws, ln < 16 ? ln * 128u : OOB, WS_PROBE + (unsigned)(opq(bid) & 7) * 2048u, AUX_SC1);
          const bool ok = ln < 16 && v[1] == tag && v[3] == tag;
          const float skew = sum16(ok ? (float)(int)(v[2] - tbase) : 0.f) * __builtin_amdgcn_rcpf(sum16(ok ? 1.f : 0.f));
          const float sum = sum16(ok ? (float)(v[0] & 0xFFFFFu) : 0.f), cn = sum16(ok ? (float)(v[0] >> 20) : 0.f);
          const float e = sum * __builtin_amdgcn_rcpf(cn) - E0_NS;
          const float x = fminf(fmaxf(fminf(e * GX1, XB + e * GX2), XMIN), XMAX) * (span * (1.f / 15200.f)) + fminf(fmaxf(skew * ALIGN, -900.f), 900.f);
          if (lane == 0) __hip_atomic_store(&lds.ext, cn > 0.f ? (int)x : 0, WG_RELAXED);
        }
        const int x = __builtin_amdgcn_readfirstlane(__hip_atomic_load(&lds.ext, WG_RELAXED));
        if (x != EXT_NONE) {
          settled = true;
          tpoll += (unsigned)x;
          del += __builtin_amdgcn_readfirstlane((int)((float)x * __builtin_amdgcn_rcpf((float)left)));
        }
      }
    }
    due += (unsigned)(left <= 2 ? del - SPRINT_NS : del);
    while ((int)((now = now_ns()) - due) < 0) __builtin_amdgcn_s_sleep(1);
    tlast = now;
  };

  auto producers = [&]() {
    const int b2 = opq(bid), w2 = opq(wave), tb = b2 >> 5;
    if (w2 == 0 && lane == 0) __builtin_amdgcn_raw_buffer_store_b32(tag, rws, 0, WS_HDR + (unsigned)b2 * 4u, AUX_SC1);
    int idx = 0;
    const unsigned z = (unsigned)opv(0);
#pragma unroll
    for (int a = 0; a < B; ++a) {
      const int ia = base[a] + 7 + 8 * mbcnt64(lead[a], z) + mbcnt64(heavy[a], z);
      idx = __builtin_amdgcn_inverse_ballot_w64(a <= tb ? lead[a] : 0ull) ? ia : idx;
    }
    const int s2 = (int)(((float)(((b2 & 7) == hs ? NSS : NSF) * (idx - w2 + 1) - 1) + 0.5f) * __builtin_amdgcn_rcpf((float)opq(total)));
    const unsigned bit = ((lvb >> tb) & 1u) << (s2 & 31), blo = s2 < 32 ? bit : 0u, bhi = s2 < 32 ? 0u : bit;
    const bool shr = ((eqb >> tb) & 1u) || members(tb) != (1u << tb);
    const u32x4 r4 = {wave_or(blo), wave_or(bhi), wave_or(shr ? blo : 0u), wave_or(shr ? bhi : 0u)};
    if (lane == 0) {
      lds.prod[w2] = r4;
      asm volatile("s_waitcnt lgkmcnt(0)" ::: "memory");
      __hip_atomic_fetch_add(&lds.ready, 1u, WG_RELAXED);
    }
  };
  unsigned long long need = 0;
  int nsrc = 0, srcv = 0, rowv = 0;
  unsigned go[2] = {0u, 0u};
  auto row_base = [&]() { const int b2 = opq(bid); return (unsigned)(((b2 & 7) * ROWS + (b2 >> 3)) * SLOTS); };
  auto slot_off = [&](int k0, int ln, unsigned rb) { return (rb + (unsigned)__builtin_amdgcn_ds_bpermute(4 * (k0 + (ln >> 3)), srcv)) * 1024u + (ln & 7) * 16u; };
  auto resolve = [&]() {
    while (__builtin_amdgcn_readfirstlane(__hip_atomic_load(&lds.ready, WG_RELAXED)) < NW) __builtin_amdgcn_s_sleep(1);
    asm volatile("" ::: "memory");
    const int ln = opv(lane), w2 = opq(wave);
    const unsigned rb = row_base();
    const u32x4 pr = lds.prod[ln & 7];
    need = or8_u64(pr[0], pr[1]);
    const unsigned long long early = or8_u64(pr[2], pr[3]);
    const int seqv = __builtin_amdgcn_ds_permute(ln < B && ((uall >> ln) & 1u) ? 4 * __builtin_popcount(uall & ((1u << (ln & 7)) - 1u)) : 252, ln);
    rowv = 4 * __builtin_amdgcn_ds_bpermute(4 * (ln >> 3), seqv) + (w2 & 3);
    nsrc = __builtin_popcountll(need);
    srcv = __builtin_amdgcn_ds_permute(__builtin_amdgcn_inverse_ballot_w64(need) ? 4 * mbcnt64(need) : 252, ln);
#pragma unroll
    for (int t = 0; t < 2; ++t) go[t] = slot_off(8 * t, ln, rb);
    return __builtin_amdgcn_inverse_ballot_w64(need & ~early) ? WS_PO + (rb + (unsigned)ln) * 1024u + w2 * 128u : WS_END;
  };
  const int ires = min(TOUCH_AT, nmine - 1);

  unsigned tch = 0;
  int i = 0;
  asm volatile("" : "+s"(i));
  do {
    pace(2 * (nmine - i) - 1);
    issue(vr, vc, voff, i);
    __builtin_amdgcn_sched_barrier(0);
    if (i == 0) producers();
    const unsigned meta = (unsigned)__builtin_amdgcn_readlane((int)myit[1], i);
    const int tok0 = (int)(meta >> 8) * BS;
    u32x4 t0, t1, t2, t3, kt[4];
    asm volatile("" ::"v"(tch));
    xchg<0>(kr[0], kr[1], t0, t1);
    xchg<0>(kr[2], kr[3], t2, t3);
    xchg<1>(t0, t2, kt[0], kt[2]);
    xchg<1>(t1, t3, kt[1], kt[3]);
    __builtin_amdgcn_sched_barrier(0);
    unsigned tcho = WS_END;
    if (i == 0 && refwg && wave == NW - 1) {
      const int ln = opv(lane);
      tcho = ln < 8 ? posto() + ln * 2048u : WS_END;
    }
    if (i >= ires && nsrc == 0) tcho = resolve();
    if (i + 1 < nmine) pace(2 * (nmine - i) - 2);
    issue(kr, kc, koff, i + 1);
    tch = __builtin_amdgcn_raw_buffer_load_b32(rws, tcho, 0, AUX_SC1);
    __builtin_amdgcn_sched_barrier(0);
    u32x2 pp[2];
#pragma unroll
    for (int hf = 0; hf < 2; ++hf) {
      if ((meta >> (4 * hf)) & 0xFu) {
        f32x4 s = {0.f, 0.f, 0.f, 0.f};
#pragma unroll
        for (int st = 0; st < 4; ++st)
          s = __builtin_amdgcn_mfma_f32_16x16x32_bf16(__builtin_bit_cast(bf16x8, kt[st]), __builtin_bit_cast(bf16x8, qf[hf][st]), s, 0, 0, 0);
        const int lim = ((meta >> (4 * hf + (n >> 2))) & 1u) ? ctl[hf] - tok0 : 0;
        float x[4], e[4];
#pragma unroll
        for (int r = 0; r < 4; ++r) x[r] = (r < lim) ? s[r] * SCL : -INFINITY;
        const float tm = xrow_max(fmaxf(fmaxf(x[0], x[1]), fmaxf(x[2], x[3])));
        const float mn = fmaxf(m[hf], tm);
        const float al = __builtin_amdgcn_exp2f(m[hf] - mn);
#pragma unroll
        for (int r = 0; r < 4; ++r) e[r] = __builtin_amdgcn_exp2f(x[r] - mn);
        l[hf] = l[hf] * al + ((e[0] + e[1]) + (e[2] + e[3]));
        m[hf] = mn;
#pragma unroll
        for (int j = 0; j < 8; ++j) o[hf][j] *= al;
        pp[hf] = u32x2{pack_bf16(e[0], e[1]), pack_bf16(e[2], e[3])};
      }
    }
    if (i + 1 >= nmine) {
#pragma unroll
      for (int hf = 0; hf < 2; ++hf) {
        const float lt = xrow_sum(l[hf]);
        if (g == 0) lds.ml[opq(wave)][hf * 16 + n] = f32x2{m[hf], lt};
      }
      asm volatile("s_waitcnt lgkmcnt(0)" ::: "memory");
      if (lane == 0) __hip_atomic_fetch_add(&lds.done, 1u, WG_RELAXED);
    }
    __builtin_amdgcn_sched_barrier(0);
    u32x2 vt[8];
#pragma unroll
    for (int w = 0; w < 4; ++w) {
      vt[2 * w] = u32x2{__builtin_amdgcn_perm(vr[1][w], vr[0][w], 0x05040100u), __builtin_amdgcn_perm(vr[3][w], vr[2][w], 0x05040100u)};
      vt[2 * w + 1] = u32x2{__builtin_amdgcn_perm(vr[1][w], vr[0][w], 0x07060302u), __builtin_amdgcn_perm(vr[3][w], vr[2][w], 0x07060302u)};
    }
#pragma unroll
    for (int hf = 0; hf < 2; ++hf) {
      if ((meta >> (4 * hf)) & 0xFu) {
#pragma unroll
        for (int j = 0; j < 8; ++j)
          o[hf][j] = __builtin_amdgcn_mfma_f32_16x16x16bf16_1k(__builtin_bit_cast(s16x4, vt[j]), __builtin_bit_cast(s16x4, pp[hf]), o[hf][j], 0, 0, 0);
      }
    }
    __builtin_amdgcn_sched_barrier(0);
  } while (++i < nmine);

#pragma unroll
  for (int hf = 0; hf < 2; ++hf) {
    const int row = hf * 16 + n;
    if ((uall >> (row >> 2)) & 1u) {
#pragma unroll
      for (int j = 0; j < 8; ++j) *reinterpret_cast<f32x4*>(&lds.u.o[wave][row][16 * j + 4 * g]) = o[hf][j];
    }
  }
  __builtin_amdgcn_s_waitcnt(0x0F70);
  asm volatile("" ::"v"(cpf), "v"(pf[0]), "v"(pf[1]), "v"(pf[2]), "v"(pf[3]), "v"(pf[4]), "v"(pf[5]));

  const int kstep = nseq > 2 ? 2 : 1, kfirst = wave >> 2;
  if (kfirst < kstep) {
    while (__builtin_amdgcn_readfirstlane(__hip_atomic_load(&lds.done, WG_RELAXED)) < NW) __builtin_amdgcn_s_sleep(1);
    asm volatile("" ::: "memory");
    const f32x2 ml = lds.ml[lane & 7][rowv & 31];
    const float M = max8(ml[0]);
    const float wt = __builtin_amdgcn_exp2f(ml[0] - M);
    const float L = sum8(wt * ml[1]);
    const int wn = (int)__float_as_uint(wt * __builtin_amdgcn_rcpf(L));
    const int lsev = (int)__float_as_uint(M + __builtin_amdgcn_logf(L));
    for (int k = kfirst;; k += kstep) {
      const int row = __builtin_amdgcn_readlane(rowv, 8 * k) & 31;
      f32x2 wp[4];
#pragma unroll
      for (int w = 0; w < NW; ++w) wp[w >> 1][w & 1] = __uint_as_float((unsigned)__builtin_amdgcn_readlane(wn, 8 * k + w));
      const unsigned lh = (unsigned)__builtin_amdgcn_readlane(lsev, 8 * k) >> (16 * (lane & 1));
      unsigned tw0 = tg8 | (lh & 0xFFu), tw1 = tg8 | ((lh >> 8) & 0xFFu);
      int e0 = (row * OROW + 2 * lane) * 4, e1 = e0 + 16 * ROWS * OROW;
      unsigned pso = WS_PO + (unsigned)((h * ROWS + row) * SLOTS + sp) * 1024u;
      asm volatile("" : "+v"(tw0), "+v"(tw1), "+v"(e0), "+v"(e1), "+s"(pso), "+v"(wp[0]), "+v"(wp[1]), "+v"(wp[2]), "+v"(wp[3]));
      if (k == kfirst) lds_barrier();
      if (k >= nseq) break;
      const char* img = reinterpret_cast<const char*>(&lds.u.o[0][0][0]);
      f32x2 ov[NW];
#pragma unroll
      for (int w = 0; w < NW; ++w) ov[w] = *reinterpret_cast<const f32x2*>(img + (w < 4 ? e0 : e1) + (w & 3) * (ROWS * OROW * 4));
      f32x2 a0 = wp[0][0] * ov[0], a1 = wp[2][0] * ov[4];
#pragma unroll
      for (int w = 1; w < 4; ++w) {
        a0 += wp[w >> 1][w & 1] * ov[w];
        a1 += wp[2 + (w >> 1)][w & 1] * ov[4 + w];
      }
      a0 += a1;
      __builtin_amdgcn_raw_buffer_store_b128(u32x4{__float_as_uint(a0[0]), tw0, __float_as_uint(a0[1]), tw1}, rws, lane * 16, pso, AUX_SC1);
      if (k + kstep >= nseq) break;
    }
  } else {
    lds_barrier();
  }
  __builtin_amdgcn_sched_barrier(0);

  if (bid == 0 && wave == NW - 2) {
    for (unsigned spins = 0;; ++spins) {
      const u32x4 hd = __builtin_amdgcn_raw_buffer_load_b128(rws, lane * 16, WS_HDR, AUX_SC1);
      const bool ok = hd[0] == tag && hd[1] == tag && hd[2] == tag && hd[3] == tag;
      if (__builtin_amdgcn_ballot_w64(!ok) == 0ull || spins > SPIN_CAP) break;
      __builtin_amdgcn_s_sleep(2);
      asm volatile("" ::: "memory");
    }
    if (lane == 0) __hip_atomic_store((gu32*)(ws + WS_EPOCH), tag, DEV_RELAXED);
  }

  u32x4 og[2];
  auto gather_init = [&](int k0) {
#pragma unroll
    for (int t = 0; t < 2; ++t) {
      const unsigned tw = (k0 + 8 * t + (lane >> 3) < nsrc) ? ~tg8 : tg8;
      og[t] = u32x4{0u, tw | ((lane & 1) ? 0x80u : 0u), 0u, tw | ((lane & 1) ? 0xFFu : 0u)};
    }
  };
  gather_init(0);
  unsigned psel = (lane & 1) ? 0x04000c0cu : 0x0c0c0400u;
  const unsigned oo = (32u * ((lane >> 1) & 3) + 16u * (lane & 1) + 8u * (lane >> 5) + wave) * 2u;
  unsigned long long op = arg(5) + (unsigned)(((ix >> 2) * H + hx * G + (ix & 3)) * D * 2) + oo;
  unsigned gso = WS_PO + wave * 128u;
  asm volatile("" : "+v"(psel), "+v"(op), "+v"(og[0]), "+v"(og[1]), "+s"(gso));
  __builtin_amdgcn_sched_barrier(0);
  if (!guest && ((need >> sp) & 1u)) __builtin_amdgcn_s_sleep(10);
  while ((int)(now_ns() - tpoll) < 0) __builtin_amdgcn_s_sleep(1);
#pragma unroll
  for (int t = 0; t < 2; ++t)
    if (8 * t + (lane >> 3) < nsrc) og[t] = __builtin_amdgcn_raw_buffer_load_b128(rws, go[t], gso, AUX_SC1);
  float Mr, Wr;
  f32x2 ar;
  auto batch = [&](bool first) {
    auto stale = [&](const u32x4& v) { return (v[1] ^ tg8) | (v[3] ^ tg8); };
    if (__builtin_expect(__builtin_amdgcn_ballot_w64((stale(og[0]) | stale(og[1])) > 0xFFu) != 0ull, 0)) {
      unsigned spins = 0;
      do {
        __builtin_amdgcn_s_sleep(3);
        asm volatile("" ::: "memory");
        if (stale(og[0]) > 0xFFu) og[0] = __builtin_amdgcn_raw_buffer_load_b128(rws, go[0], gso, AUX_SC1);
        if (stale(og[1]) > 0xFFu) og[1] = __builtin_amdgcn_raw_buffer_load_b128(rws, go[1], gso, AUX_SC1);
      } while (__builtin_amdgcn_ballot_w64((stale(og[0]) | stale(og[1])) > 0xFFu) != 0ull && ++spins < SPIN_CAP);
    }
    const unsigned h0 = __builtin_amdgcn_perm(og[0][3], og[0][1], psel), h1 = __builtin_amdgcn_perm(og[1][3], og[1][1], psel);
    const float l0 = __uint_as_float(h0 | dpp<XOR1>(h0)), l1 = __uint_as_float(h1 | dpp<XOR1>(h1));
    const float M = group_max(l0, first ? l1 : fmaxf(l1, Mr));
    const float w0 = __builtin_amdgcn_exp2f(l0 - M), w1 = __builtin_amdgcn_exp2f(l1 - M);
    const float wsum = w0 + w1;
    const f32x2 part = {fmaf(w0, __uint_as_float(og[0][0]), w1 * __uint_as_float(og[1][0])), fmaf(w0, __uint_as_float(og[0][2]), w1 * __uint_as_float(og[1][2]))};
    if (first) {
      Wr = wsum;
      ar = part;
    } else {
      const float wr = __builtin_amdgcn_exp2f(Mr - M);
      Wr = Wr * wr + wsum;
      ar = ar * wr + part;
    }
    Mr = M;
  };
  batch(true);
  for (int k0 = 16; __builtin_expect(k0 < nsrc, 0); k0 += 16) {
    gather_init(k0);
#pragma unroll
    for (int t = 0; t < 2; ++t) go[t] = slot_off(k0 + 8 * t, lane, row_base());
    batch(false);
  }
  const auto sw = __builtin_amdgcn_permlane32_swap(__float_as_uint(ar[0]), __float_as_uint(ar[1]), false, false);
  const auto ww = __builtin_amdgcn_permlane32_swap(__float_as_uint(Wr), __float_as_uint(Wr), false, false);
  const float z0 = __uint_as_float(sw[0]) + __uint_as_float(sw[1]), w2 = __uint_as_float(ww[0]) + __uint_as_float(ww[1]);
  const auto s16 = __builtin_amdgcn_permlane16_swap(__float_as_uint(z0), __float_as_uint(w2), false, false);
  float y = __uint_as_float(s16[0]) + __uint_as_float(s16[1]);
  y += dppf<ROR8>(y);
  const auto f16 = __builtin_amdgcn_permlane16_swap(__float_as_uint(y), __float_as_uint(y), false, false);
  const unsigned pk = pack_bf16(__uint_as_float(f16[0]) * __builtin_amdgcn_rcpf(__uint_as_float(f16[1])), 0.f);
  if (__builtin_amdgcn_inverse_ballot_w64(0x000000FF000000FFull)) *(__attribute__((address_space(1))) unsigned short*)op = (unsigned short)pk;
}

}

extern "C" void kernel_launch(void* const* d_in, const int* in_sizes, int n_in, void* d_out, int out_size, void* d_ws,
                              size_t ws_size, hipStream_t stream) {
  const unsigned cache_bytes = (unsigned)in_sizes[1] * 2u;
  attn_paged<<<NWG, NT, 0, stream>>>(d_in[0], d_in[1], d_in[2], d_in[3], d_in[4], d_out, d_ws, cache_bytes, 0u);
}
```

```cpp
#include <hip/hip_runtime.h>
#include <hip/hip_bf16.h>
#include <cstdint>

namespace {

constexpr int B = 8, H = 32, HKV = 8, G = H / HKV, D = 128, BS = 16, NB = 512, LK = NB * BS;
constexpr int NWG = 256, NW = 8, NT = NW * 64;
constexpr int ROWS = B * G;
constexpr int PAGE_BYTES = BS * HKV * D * 2;
constexpr int TOK_BYTES = HKV * D * 2;
constexpr float SCL = 0.08838834764831845f * 1.4426950408889634f;
constexpr float MINIT = -1e30f;

constexpr int GUESTS = 2;
constexpr int NSF = 32 - GUESTS, NSS = 32 + 3 * GUESTS;
constexpr int SLOTS = 48;
constexpr int MAX_ITEMS = 24;
constexpr int WARM = 2;
constexpr int CODE_LINES = 223;

#define PACE 66.5f
#define T0_NS 2000
#define EARLY_NS 290.0f
#define GUEST_NS 300.0f
#define SPRINT_NS 170
#define POLL_NS 650
#define FA 0.23f
#define FB 0.36f
#define ALIGN 1.0f
#define TLBW 1
#define TLBO 1
#define B2_NS 420
#define TP_NS 300
#define TC_NS 1300
#define E0_NS 800.0f
#define GX1 5.0f
#define XB 450.0f
#define GX2 2.5f
#define XMIN 0.0f
#define XMAX 2300.0f
constexpr int LEAD = 4, TOUCH_AT = 3, REF_A = 14, REF_B = 23;
constexpr unsigned SPIN_CAP = 1u << 20;

constexpr unsigned WS_EPOCH = 0;
constexpr unsigned WS_HDR = 4096;
constexpr unsigned WS_PROBE = 8192;
constexpr unsigned WS_PO = WS_PROBE + 128 * 128;
constexpr unsigned WS_END = WS_PO + HKV * ROWS * SLOTS * 1024;

constexpr int AUX_NT = 2, AUX_SC1 = 16;
constexpr unsigned OOB = 0x80000000u;

using u32x4 = __attribute__((ext_vector_type(4))) unsigned;
using u32x16 = __attribute__((ext_vector_type(16))) unsigned;
using i32x8 = __attribute__((ext_vector_type(8))) int;
using u32x2 = __attribute__((ext_vector_type(2))) unsigned;
using f32x4 = __attribute__((ext_vector_type(4))) float;
using f32x2 = __attribute__((ext_vector_type(2))) float;
using bf16x8 = __attribute__((ext_vector_type(8))) __bf16;
using bf16x2 = __attribute__((ext_vector_type(2))) __bf16;
using s16x4 = __attribute__((ext_vector_type(4))) short;
typedef __attribute__((address_space(1))) unsigned gu32;
typedef __attribute__((address_space(1))) char gchar;
typedef __attribute__((address_space(1))) u32x4 gu32x4;

constexpr int OROW = D + 4, BTW = 68, QROW = 17;
struct Lds {
  union {
    struct {
      u32x4 q[ROWS][QROW];
      int bt[B][NW][BTW];
    } pro;
    float o[NW][ROWS][OROW];
  } u;
  f32x2 ml[NW][ROWS];
  u32x2 items[NW][MAX_ITEMS];
  u32x4 prod[NW];
  u32x2 own[NW];
  unsigned tag;
  unsigned done;
  unsigned ready;
  unsigned lat;
  int ext;
  unsigned claim;
  unsigned pad[2];
};
constexpr int EXT_NONE = -0x40000000;

#define WG_RELAXED __ATOMIC_RELAXED, __HIP_MEMORY_SCOPE_WORKGROUP
#define DEV_RELAXED __ATOMIC_RELAXED, __HIP_MEMORY_SCOPE_AGENT

__device__ __forceinline__ __amdgpu_buffer_rsrc_t make_rsrc(const void* p, unsigned bytes) {
  return __builtin_amdgcn_make_buffer_rsrc((void*)p, 0, bytes, 0x00020000);
}
__device__ __forceinline__ void lds_barrier() { asm volatile("s_waitcnt lgkmcnt(0)\n\ts_barrier" ::: "memory"); }
__device__ __forceinline__ unsigned now_ns() { return (unsigned)__builtin_amdgcn_s_memrealtime() * 10u; }
__device__ __forceinline__ bool loads_pending() { return (__builtin_amdgcn_s_getreg(7 | (3 << 11)) & 15) != 0; }

template <int CTRL>
__device__ __forceinline__ unsigned dpp(unsigned x) {
  return (unsigned)__builtin_amdgcn_mov_dpp((int)x, CTRL, 0xf, 0xf, true);
}
template <int CTRL>
__device__ __forceinline__ float dppf(float x) {
  return __uint_as_float(dpp<CTRL>(__float_as_uint(x)));
}
constexpr int XOR1 = 0xB1, XOR2 = 0x4E, HMIR = 0x141, ROR8 = 0x128;

__device__ __forceinline__ float xrow_max(float x) {
  auto s = __builtin_amdgcn_permlane16_swap(__float_as_uint(x), __float_as_uint(x), false, false);
  x = fmaxf(__uint_as_float(s[0]), __uint_as_float(s[1]));
  auto t = __builtin_amdgcn_permlane32_swap(__float_as_uint(x), __float_as_uint(x), false, false);
  return fmaxf(__uint_as_float(t[0]), __uint_as_float(t[1]));
}
__device__ __forceinline__ float xrow_sum(float x) {
  auto s = __builtin_amdgcn_permlane16_swap(__float_as_uint(x), __float_as_uint(x), false, false);
  x = __uint_as_float(s[0]) + __uint_as_float(s[1]);
  auto t = __builtin_amdgcn_permlane32_swap(__float_as_uint(x), __float_as_uint(x), false, false);
  return __uint_as_float(t[0]) + __uint_as_float(t[1]);
}
__device__ __forceinline__ float group_max(float a, float b) {
  float r, t;
  asm volatile(
      "v_max_f32 %0, %2, %3\n\ts_nop 1\n\t"
      "v_max_f32_dpp %0, %0, %0 row_ror:8 row_mask:0xf bank_mask:0xf\n\t"
      "v_mov_b32 %1, %0\n\ts_nop 1\n\t"
      "v_permlane16_swap_b32 %0, %1\n\t"
      "v_max_f32 %0, %0, %1\n\t"
      "v_mov_b32 %1, %0\n\ts_nop 1\n\t"
      "v_permlane32_swap_b32 %0, %1\n\t"
      "v_max_f32 %0, %0, %1"
      : "=&v"(r), "=&v"(t)
      : "v"(a), "v"(b));
  return r;
}
__device__ __forceinline__ float max8(float x) {
  x = fmaxf(x, dppf<XOR1>(x));
  x = fmaxf(x, dppf<XOR2>(x));
  return fmaxf(x, dppf<HMIR>(x));
}
__device__ __forceinline__ float sum8(float x) {
  x += dppf<XOR1>(x);
  x += dppf<XOR2>(x);
  return x + dppf<HMIR>(x);
}
__device__ __forceinline__ float sum16(float x) {
  x = sum8(x);
  return x + dppf<ROR8>(x);
}
__device__ __forceinline__ unsigned or8(unsigned x) {
  x |= dpp<XOR1>(x);
  x |= dpp<XOR2>(x);
  return x | dpp<HMIR>(x);
}
__device__ __forceinline__ unsigned wave_or(unsigned x) {
  x = or8(x);
  x |= dpp<ROR8>(x);
  auto s = __builtin_amdgcn_permlane16_swap(x, x, false, false);
  x = s[0] | s[1];
  auto t = __builtin_amdgcn_permlane32_swap(x, x, false, false);
  return t[0] | t[1];
}
__device__ __forceinline__ unsigned long long or8_u64(unsigned lo, unsigned hi) {
  return (unsigned)__builtin_amdgcn_readfirstlane(or8(lo)) | ((unsigned long long)(unsigned)__builtin_amdgcn_readfirstlane(or8(hi)) << 32);
}
__device__ __forceinline__ unsigned pack_bf16(float lo, float hi) {
  return __builtin_bit_cast(unsigned, __builtin_convertvector(f32x2{lo, hi}, bf16x2));
}
__device__ __forceinline__ int mbcnt64(unsigned long long m, unsigned z = 0u) {
  return (int)__builtin_amdgcn_mbcnt_hi((unsigned)(m >> 32), __builtin_amdgcn_mbcnt_lo((unsigned)m, z));
}

#define XCHG_BODY(QP)                                                                 \
  "v_cndmask_b32_dpp %0, %12, %8, vcc " QP " row_mask:0xf bank_mask:0xf\n\t"          \
  "v_cndmask_b32_dpp %1, %13, %9, vcc " QP " row_mask:0xf bank_mask:0xf\n\t"          \
  "v_cndmask_b32_dpp %2, %14, %10, vcc " QP " row_mask:0xf bank_mask:0xf\n\t"         \
  "v_cndmask_b32_dpp %3, %15, %11, vcc " QP " row_mask:0xf bank_mask:0xf\n\t"         \
  "s_not_b64 vcc, vcc\n\t"                                                            \
  "v_cndmask_b32_dpp %4, %8, %12, vcc " QP " row_mask:0xf bank_mask:0xf\n\t"          \
  "v_cndmask_b32_dpp %5, %9, %13, vcc " QP " row_mask:0xf bank_mask:0xf\n\t"          \
  "v_cndmask_b32_dpp %6, %10, %14, vcc " QP " row_mask:0xf bank_mask:0xf\n\t"         \
  "v_cndmask_b32_dpp %7, %11, %15, vcc " QP " row_mask:0xf bank_mask:0xf"
#define XCHG_OPS                                                                              \
  : "=&v"(x0), "=&v"(x1), "=&v"(x2), "=&v"(x3), "=&v"(y0), "=&v"(y1), "=&v"(y2), "=&v"(y3)    \
  : "v"(a[0]), "v"(a[1]), "v"(a[2]), "v"(a[3]), "v"(b[0]), "v"(b[1]), "v"(b[2]), "v"(b[3])    \
  : "vcc", "scc"
template <int BIT>
__device__ __forceinline__ void xchg(const u32x4& a, const u32x4& b, u32x4& x, u32x4& y) {
  unsigned x0, x1, x2, x3, y0, y1, y2, y3;
  if constexpr (BIT == 0)
    asm volatile("s_mov_b32 vcc_lo, 0x55555555\n\ts_mov_b32 vcc_hi, 0x55555555\n\t" XCHG_BODY("quad_perm:[1,0,3,2]") XCHG_OPS);
  else
    asm volatile("s_mov_b32 vcc_lo, 0x33333333\n\ts_mov_b32 vcc_hi, 0x33333333\n\t" XCHG_BODY("quad_perm:[2,3,0,1]") "\n\ts_nop 1" XCHG_OPS);
  x = u32x4{x0, x1, x2, x3};
  y = u32x4{y0, y1, y2, y3};
}

__global__ void __launch_bounds__(NT, 2)
attn_paged(const void* q_arg, const void* k_arg, const void* v_arg, const void* bt_arg, const void* ctx_arg, void* out_arg,
           void* ws_arg, unsigned cache_bytes_arg, unsigned zero_arg) {
  __shared__ __attribute__((aligned(16))) Lds lds;
  u32x16 ka;
  asm volatile("s_load_dwordx16 %0, %1, 0x0" : "=s"(ka) : "s"(__builtin_amdgcn_kernarg_segment_ptr()));
  const int tid = threadIdx.x, lane = tid & 63;
  const int wave = __builtin_amdgcn_readfirstlane(tid >> 6);
  const int n = lane & 15, g = lane >> 4;
  const int bid = blockIdx.x;
  const int hx = bid & 7, ix = bid >> 3;
  const int col = NW * lane + wave;

  unsigned cpf;
  {
    unsigned long long pc;
    asm volatile("s_getpc_b64 %0" : "=s"(pc));
    cpf = *(const gu32*)((pc & ~63ull) + (tid < CODE_LINES ? tid : 0) * 64u);
  }
  unsigned bto = (unsigned)(wave * NB * 4 + lane * 16);
  unsigned wvo = (unsigned)((lane >> 5) * PAGE_BYTES + ((lane & 31) >> 1) * TOK_BYTES + (lane & 1) * 128) +
                 (unsigned)(ix * 3 * WARM) * (unsigned)PAGE_BYTES + (unsigned)(hx * D * 2);
  unsigned tvk = ((unsigned)(ix + 32 * lane) << 21) + (unsigned)bid * 4352u;
  unsigned tvw = WS_PO + (unsigned)((((hx & 4) | (lane & 3)) * ROWS + ix) * SLOTS + SLOTS - 1) * 1024u;
  unsigned tvo = (unsigned)(((ix >> 2) * H + hx * G + (ix & 3)) * D * 2);
  tvk = lane < 2 ? tvk : OOB;
  tvw = lane < 4 ? tvw : OOB;
  tvo = lane < 1 ? tvo : OOB;
  asm volatile("" : "+v"(wvo), "+v"(tvk), "+v"(tvw), "+v"(tvo));
  tvk = tid >= 448 ? wvo : tvk;
  asm volatile("" : "+v"(tvk));
  unsigned xv0 = tid >= 384 ? tvk : tvw, xv1 = tid >= 384 ? tvk : tvo;
  asm volatile("s_waitcnt lgkmcnt(0)" : "+s"(ka), "+v"(bto), "+v"(xv0), "+v"(xv1));
  auto arg = [&](int k) { return (unsigned long long)ka[2 * k] | ((unsigned long long)ka[2 * k + 1] << 32); };
  const gchar* const q = (const gchar*)arg(0);
  const char* const kc = (const char*)(const gchar*)arg(1);
  const char* const vc = (const char*)(const gchar*)arg(2);
  const gchar* const bt = (const gchar*)arg(3);
  const char* const out = (const char*)(const gchar*)arg(5);
  gchar* const ws = (gchar*)arg(6);
  const unsigned cache_bytes = ka[14];

  const u32x4 bt0 = *(const gu32x4*)(bt + bto);
  const u32x4 bt1 = *(const gu32x4*)(bt + bto + 1024);
  const i32x8 cv = *(const __attribute__((address_space(4))) i32x8*)arg(4);
  unsigned long long t64 = __builtin_amdgcn_s_memrealtime();
  int cl[B];
#pragma unroll
  for (int b = 0; b < B; ++b) cl[b] = cv[b];
  __builtin_amdgcn_sched_barrier(0);
  asm volatile("s_barrier" ::: "memory");

  unsigned pf[6];
  auto warm = [&](int slot) {
    const unsigned nrec = wave == NW - 1 ? cache_bytes : 0u;
    pf[2 * slot] = __builtin_amdgcn_raw_buffer_load_b32(make_rsrc(kc, nrec), wvo, slot * WARM * PAGE_BYTES, 0);
    pf[2 * slot + 1] = __builtin_amdgcn_raw_buffer_load_b32(make_rsrc(vc, nrec), wvo, slot * WARM * PAGE_BYTES, 0);
    __builtin_amdgcn_sched_barrier(0);
  };
  static_assert(WARM == 2, "one load instruction covers two pages");
  {
    const bool aux = TLBO && wave == NW - 3;
    const unsigned nrec = wave >= NW - 1 - TLBW ? cache_bytes : 0u;
    pf[0] = __builtin_amdgcn_raw_buffer_load_b32(make_rsrc(aux ? (const char*)ws : kc, aux ? WS_END : nrec), xv0, 0, 0);
    pf[1] = __builtin_amdgcn_raw_buffer_load_b32(make_rsrc(aux ? out : vc, aux ? B * H * D * 2u : nrec), xv1, 0, 0);
    __builtin_amdgcn_sched_barrier(0);
  }
  const int hs = (hx & 4) | ((1 - (int)(ka[2] >> 8)) & 3);
  const bool guest = hx != hs && ix >= NSF;
  int h = guest ? hs : hx;
  const int spg = ((hx - hs - 1) & 3) * GUESTS - NSF, spr = hx == hs ? 3 * GUESTS : 0;
  int sp = ix + (guest ? spg : spr);
  asm volatile("" : "+s"(h), "+s"(sp));
  u32x4 qv = *(const gu32x4*)(q + (unsigned)((wave * H + h * G) * D * 2) + lane * 16u);
  const unsigned epoch = __hip_atomic_load((gu32*)(ws + WS_EPOCH), DEV_RELAXED);
  __builtin_amdgcn_sched_barrier(0);
  unsigned koff[2], voff[2];
#pragma unroll
  for (int t = 0; t < 2; ++t) {
    koff[t] = (unsigned)((4 * (n >> 2) + 2 * t) * TOK_BYTES + g * 64 + (n & 3) * 16);
    voff[t] = (unsigned)((4 * g + 2 * t) * TOK_BYTES + n * 16);
  }
  f32x4 o[2][8];
  float m[2], l[2];
#pragma unroll
  for (int hf = 0; hf < 2; ++hf) {
    m[hf] = MINIT;
    l[hf] = 0.f;
#pragma unroll
    for (int j = 0; j < 8; ++j) o[hf][j] = f32x4{0.f, 0.f, 0.f, 0.f};
    asm volatile("" : "+v"(o[hf][0]), "+v"(o[hf][1]), "+v"(o[hf][2]), "+v"(o[hf][3]), "+v"(o[hf][4]), "+v"(o[hf][5]), "+v"(o[hf][6]), "+v"(o[hf][7]),
                 "+v"(m[hf]), "+v"(l[hf]), "+v"(koff[hf]), "+v"(voff[hf]));
  }
  __builtin_amdgcn_sched_barrier(0);
  asm volatile("" : "+s"(t64) : "s"(cl[0]));
  const unsigned tstart = (unsigned)t64;
  __builtin_amdgcn_sched_barrier(0);
  warm(1);
  asm volatile("" : "+s"(cl[0]), "+s"(cl[1]), "+s"(cl[2]), "+s"(cl[3]), "+s"(cl[4]), "+s"(cl[5]), "+s"(cl[6]), "+s"(cl[7]));

  int cw = cl[0];
#pragma unroll
  for (int b = 1; b < B; ++b) cw = (wave == b) ? cl[b] : cw;
  unsigned long long live[B];
  unsigned lvb = 0;
#pragma unroll
  for (int b = 0; b < B; ++b) {
    cl[b] = (cl[b] <= 0 || cl[b] > LK) ? LK : cl[b];
    live[b] = __builtin_amdgcn_ballot_w64(col * BS < cl[b]);
    lvb |= (col * BS < cl[b]) ? (1u << b) : 0u;
  }
  int ctl[2];
#pragma unroll
  for (int hf = 0; hf < 2; ++hf) {
    int c = cl[4 * hf];
#pragma unroll
    for (int a = 1; a < 4; ++a) c = ((n >> 2) == a) ? cl[4 * hf + a] : c;
    ctl[hf] = c - 4 * g;
  }
  asm volatile("" : "+v"(lvb), "+v"(ctl[0]), "+v"(ctl[1]));
#pragma unroll
  for (int e = 0; e < 4; ++e) {
    lds.u.pro.bt[wave][4 * (lane & 1) + e][lane >> 1] = (int)bt0[e];
    lds.u.pro.bt[wave][4 * (lane & 1) + e][32 + (lane >> 1)] = (int)bt1[e];
  }
  lds_barrier();
  warm(2);

  int pv[B];
#pragma unroll
  for (int b = 0; b < B; ++b) pv[b] = lds.u.pro.bt[b][wave][lane];
  unsigned long long lead[B], heavy[B];
  unsigned eqb = 0;
  {
    unsigned long long eq[B];
#pragma unroll
    for (int b = 0; b < B; ++b) {
      pv[b] = (col * BS < cl[b]) ? pv[b] : (b == 0 ? (int)0x80000000 : pv[b - 1]);
      eq[b] = b == 0 ? 0ull : __builtin_amdgcn_ballot_w64(pv[b] == pv[b - 1]);
      if (b) eqb |= (pv[b] == pv[b - 1]) ? (1u << b) : 0u;
    }
    unsigned long long fol = 0;
#pragma unroll
    for (int b = B - 1; b >= 0; --b) {
      lead[b] = live[b] & ~eq[b];
      heavy[b] = lead[b] & fol;
      fol = eq[b] & (live[b] | fol);
    }
  }
  asm volatile("" : "+v"(eqb));
  auto members = [&](int b) { return (((2u << __builtin_ctz(~(eqb >> (b + 1)))) - 1u) << b) & lvb; };
  int base[B + 1], cnt = 0;
  base[0] = 0;
#pragma unroll
  for (int b = 0; b < B; ++b) {
    const int c = __builtin_popcountll(lead[b]);
    cnt += c;
    base[b + 1] = base[b] + 8 * c + __builtin_popcountll(heavy[b]);
  }
  const int total = base[B];
  const unsigned ta = (unsigned)(total * sp);
  const unsigned mg = h == hs ? 0xFFFFFFFFu / NSS + 1u : 0xFFFFFFFFu / NSF + 1u;
  const int lo = (int)__umulhi(ta, mg) + wave, hi = (int)__umulhi(ta + (unsigned)total, mg) + wave;
  int nmine = 0;
#pragma unroll
  for (int b = 0; b < B; ++b) {
    if (base[b] + 7 < hi && base[b + 1] > lo) {
      const int pos = base[b] + 7 - lo + 8 * mbcnt64(lead[b]) + mbcnt64(heavy[b]);
      const unsigned long long sel = lead[b] & __builtin_amdgcn_ballot_w64((unsigned)pos < (unsigned)(hi - lo));
      if (__builtin_amdgcn_inverse_ballot_w64(sel)) lds.items[wave][nmine + mbcnt64(sel)] = u32x2{(unsigned)pv[b], members(b) | ((unsigned)col << 8)};
      nmine += __builtin_popcountll(sel);
    }
  }
  __builtin_amdgcn_fence(__ATOMIC_RELEASE, "wavefront");
  __builtin_amdgcn_wave_barrier();
  __builtin_amdgcn_fence(__ATOMIC_ACQUIRE, "wavefront");
  u32x2 myit = lds.items[wave][lane < MAX_ITEMS ? lane : 0];

  const unsigned hoff = (unsigned)(h * D * 2);
  u32x4 kr[4], vr[4];
  auto issue = [&](u32x4 (&r)[4], const char* src, const unsigned (&off)[2], int i) {
    const unsigned phys = (unsigned)__builtin_amdgcn_readlane((int)myit[0], i);
    const __amdgpu_buffer_rsrc_t rs = make_rsrc(src, i < nmine ? cache_bytes : 0u);
    const unsigned so = phys * (unsigned)PAGE_BYTES + hoff;
#pragma unroll
    for (int t = 0; t < 4; ++t) r[t] = __builtin_amdgcn_raw_buffer_load_b128(rs, off[t >> 1] + TOK_BYTES * (t & 1), so, AUX_NT);
  };
  issue(kr, kc, koff, 0);
  __builtin_amdgcn_sched_barrier(0);

  myit[1] = lane < nmine ? myit[1] : 0u;
  {
    const unsigned um = wave_or(myit[1] & 0xFFu);
    if (lane == 0) lds.own[wave] = u32x2{um, (unsigned)cnt};
  }
  if (cw <= 0) qv = u32x4{0u, 0u, 0u, 0u};
  lds.u.pro.q[wave * G + g][n] = qv;
  if (wave == 0 && lane == 0) {
    lds.tag = epoch + 1u;
    lds.done = 0u;
    lds.ready = 0u;
    lds.lat = 0u;
    lds.ext = EXT_NONE;
    lds.claim = 0u;
  }
  lds_barrier();
  const unsigned tready = now_ns();
  u32x4 qf[2][4];
#pragma unroll
  for (int hf = 0; hf < 2; ++hf)
#pragma unroll
    for (int s = 0; s < 4; ++s) qf[hf][s] = lds.u.pro.q[hf * 16 + n][4 * g + s];
  const u32x2 ownv = lds.own[lane & 7];
  const unsigned uall = __builtin_amdgcn_readfirstlane(or8(ownv[0]));
  const int nseq = __builtin_popcount(uall);
  const unsigned tag = __builtin_amdgcn_readfirstlane(lds.tag), tg8 = tag << 8;
  const __amdgpu_buffer_rsrc_t rws = make_rsrc((const char*)ws, WS_END);
  auto opq = [&](int x) { asm volatile("" : "+s"(x)); return x; };
  auto opv = [&](int x) { asm volatile("" : "+v"(x)); return x; };

  const float span = 0.125f * PACE * sum8((float)ownv[1]);
  const float dur = fmaxf(span - EARLY_NS * (float)(nseq - 1) - (guest ? GUEST_NS : 0.f), 0.f);
  int del = __builtin_amdgcn_readfirstlane((int)(dur * __builtin_amdgcn_rcpf((float)(2 * nmine + (nmine == 0))))) & -(nmine > 0);
  const unsigned tb0 = tstart * 10u + T0_NS, tb1 = tready - B2_NS, tbase = (int)(tb1 - tb0) > 0 ? tb1 : tb0;
  unsigned due = tbase + (unsigned)(((wave - LEAD) * del) >> 3);
  unsigned tpoll = tbase + POLL_NS + (unsigned)__builtin_amdgcn_readfirstlane((int)span);
  const unsigned tma = tbase + (unsigned)__builtin_amdgcn_readfirstlane((int)(span * FA));
  const unsigned tmb = tbase + (unsigned)__builtin_amdgcn_readfirstlane((int)(span * FB));
  const bool refwg = ix == REF_A || ix == REF_B;
  auto posto = [&]() { const int b2 = opq(bid); return WS_PROBE + (unsigned)((b2 & 7) + ((b2 >> 3) == REF_B ? 8 : 0)) * 128u; };
  auto claim = [&](unsigned bit) {
    unsigned old = 0;
    if (lane == 0) old = __hip_atomic_fetch_or(&lds.claim, bit, WG_RELAXED);
    return (__builtin_amdgcn_readfirstlane(old) & bit) == 0u;
  };
  bool settled = false;
  unsigned tlast = 0, topen = 0;
  auto pace = [&](int left) {
    unsigned now = now_ns();
    if (!settled && (int)(now - tma) >= 0) {
      if (topen) {
        if (lane == 0) __hip_atomic_fetch_add(&lds.lat, min(now - topen - 120u, 2500u) + (1u << 20), WG_RELAXED);
        topen = 0;
      }
      if ((int)(now - tmb) < 0) {
        if (refwg) {
          bool pend;
          for (;;) {
            now = now_ns();
            pend = loads_pending();
            if (!pend || (int)(now - (due + del)) >= 0) break;
            __builtin_amdgcn_s_sleep(1);
          }
          if (pend) topen = tlast | 1u;
          else if (lane == 0) __hip_atomic_fetch_add(&lds.lat, min(now - tlast, 2500u) + (1u << 20), WG_RELAXED);
        }
      } else {
        if (refwg && (int)(now - (tmb + TP_NS)) >= 0 && claim(1u)) {
          const int ln = opv(lane);
          const unsigned acc = __hip_atomic_load(&lds.lat, WG_RELAXED);
          __builtin_amdgcn_raw_buffer_store_b128(u32x4{acc, tag, tbase, tag}, rws, (ln >> 3) * 2048 + (ln & 7) * 16, posto(), AUX_SC1);
        } else if ((int)(now - (tmb + TC_NS)) >= 0 && claim(2u)) {
          const int ln = opv(lane);
          const u32x4 v = __builtin_amdgcn_raw_buffer_load_b128(rws, ln < 16 ? ln * 128u : OOB, WS_PROBE + (unsigned)(opq(bid) & 7) * 2048u, AUX_SC1);
          const bool ok = ln < 16 && v[1] == tag && v[3] == tag;
          const float skew = sum16(ok ? (float)(int)(v[2] - tbase) : 0.f) * __builtin_amdgcn_rcpf(sum16(ok ? 1.f : 0.f));
          const float sum = sum16(ok ? (float)(v[0] & 0xFFFFFu) : 0.f), cn = sum16(ok ? (float)(v[0] >> 20) : 0.f);
          const float e = sum * __builtin_amdgcn_rcpf(cn) - E0_NS;
          const float x = fminf(fmaxf(fminf(e * GX1, XB + e * GX2), XMIN), XMAX) * (span * (1.f / 15200.f)) + fminf(fmaxf(skew * ALIGN, -900.f), 900.f);
          if (lane == 0) __hip_atomic_store(&lds.ext, cn > 0.f ? (int)x : 0, WG_RELAXED);
        }
        const int x = __builtin_amdgcn_readfirstlane(__hip_atomic_load(&lds.ext, WG_RELAXED));
        if (x != EXT_NONE) {
          settled = true;
          tpoll += (unsigned)x;
          del += __builtin_amdgcn_readfirstlane((int)((float)x * __builtin_amdgcn_rcpf((float)left)));
        }
      }
    }
    due += (unsigned)(left <= 2 ? del - SPRINT_NS : del);
    while ((int)((now = now_ns()) - due) < 0) __builtin_amdgcn_s_sleep(1);
    tlast = now;
  };

  auto producers = [&]() {
    const int b2 = opq(bid), w2 = opq(wave), tb = b2 >> 5;
    if (w2 == 0 && lane == 0) __builtin_amdgcn_raw_buffer_store_b32(tag, rws, 0, WS_HDR + (unsigned)b2 * 4u, AUX_SC1);
    int idx = 0;
    const unsigned z = (unsigned)opv(0);
#pragma unroll
    for (int a = 0; a < B; ++a) {
      const int ia = base[a] + 7 + 8 * mbcnt64(lead[a], z) + mbcnt64(heavy[a], z);
      idx = __builtin_amdgcn_inverse_ballot_w64(a <= tb ? lead[a] : 0ull) ? ia : idx;
    }
    const int s2 = (int)(((float)(((b2 & 7) == hs ? NSS : NSF) * (idx - w2 + 1) - 1) + 0.5f) * __builtin_amdgcn_rcpf((float)opq(total)));
    const unsigned bit = ((lvb >> tb) & 1u) << (s2 & 31), blo = s2 < 32 ? bit : 0u, bhi = s2 < 32 ? 0u : bit;
    const bool shr = ((eqb >> tb) & 1u) || members(tb) != (1u << tb);
    const u32x4 r4 = {wave_or(blo), wave_or(bhi), wave_or(shr ? blo : 0u), wave_or(shr ? bhi : 0u)};
    if (lane == 0) {
      lds.prod[w2] = r4;
      asm volatile("s_waitcnt lgkmcnt(0)" ::: "memory");
      __hip_atomic_fetch_add(&lds.ready, 1u, WG_RELAXED);
    }
  };
  unsigned long long need = 0;
  int nsrc = 0, srcv = 0, rowv = 0;
  unsigned go[2] = {0u, 0u};
  auto row_base = [&]() { const int b2 = opq(bid); return (unsigned)(((b2 & 7) * ROWS + (b2 >> 3)) * SLOTS); };
  auto slot_off = [&](int k0, int ln, unsigned rb) { return (rb + (unsigned)__builtin_amdgcn_ds_bpermute(4 * (k0 + (ln >> 3)), srcv)) * 1024u + (ln & 7) * 16u; };
  auto resolve = [&]() {
    while (__builtin_amdgcn_readfirstlane(__hip_atomic_load(&lds.ready, WG_RELAXED)) < NW) __builtin_amdgcn_s_sleep(1);
    asm volatile("" ::: "memory");
    const int ln = opv(lane), w2 = opq(wave);
    const unsigned rb = row_base();
    const u32x4 pr = lds.prod[ln & 7];
    need = or8_u64(pr[0], pr[1]);
    const unsigned long long early = or8_u64(pr[2], pr[3]);
    const int seqv = __builtin_amdgcn_ds_permute(ln < B && ((uall >> ln) & 1u) ? 4 * __builtin_popcount(uall & ((1u << (ln & 7)) - 1u)) : 252, ln);
    rowv = 4 * __builtin_amdgcn_ds_bpermute(4 * (ln >> 3), seqv) + (w2 & 3);
    nsrc = __builtin_popcountll(need);
    srcv = __builtin_amdgcn_ds_permute(__builtin_amdgcn_inverse_ballot_w64(need) ? 4 * mbcnt64(need) : 252, ln);
#pragma unroll
    for (int t = 0; t < 2; ++t) go[t] = slot_off(8 * t, ln, rb);
    return __builtin_amdgcn_inverse_ballot_w64(need & ~early) ? WS_PO + (rb + (unsigned)ln) * 1024u + w2 * 128u : WS_END;
  };
  const int ires = min(TOUCH_AT, nmine - 1);

  unsigned tch = 0;
  int i = 0;
  asm volatile("" : "+s"(i));
  do {
    pace(2 * (nmine - i) - 1);
    issue(vr, vc, voff, i);
    __builtin_amdgcn_sched_barrier(0);
    if (i == 0) producers();
    const unsigned meta = (unsigned)__builtin_amdgcn_readlane((int)myit[1], i);
    const int tok0 = (int)(meta >> 8) * BS;
    u32x4 t0, t1, t2, t3, kt[4];
    asm volatile("" ::"v"(tch));
    xchg<0>(kr[0], kr[1], t0, t1);
    xchg<0>(kr[2], kr[3], t2, t3);
    xchg<1>(t0, t2, kt[0], kt[2]);
    xchg<1>(t1, t3, kt[1], kt[3]);
    __builtin_amdgcn_sched_barrier(0);
    unsigned tcho = WS_END;
    if (i == 0 && refwg && wave == NW - 1) {
      const int ln = opv(lane);
      tcho = ln < 8 ? posto() + ln * 2048u : WS_END;
    }
    if (i >= ires && nsrc == 0) tcho = resolve();
    if (i + 1 < nmine) pace(2 * (nmine - i) - 2);
    issue(kr, kc, koff, i + 1);
    tch = __builtin_amdgcn_raw_buffer_load_b32(rws, tcho, 0, AUX_SC1);
    __builtin_amdgcn_sched_barrier(0);
    u32x2 pp[2];
#pragma unroll
    for (int hf = 0; hf < 2; ++hf) {
      if ((meta >> (4 * hf)) & 0xFu) {
        f32x4 s = {0.f, 0.f, 0.f, 0.f};
#pragma unroll
        for (int st = 0; st < 4; ++st)
          s = __builtin_amdgcn_mfma_f32_16x16x32_bf16(__builtin_bit_cast(bf16x8, kt[st]), __builtin_bit_cast(bf16x8, qf[hf][st]), s, 0, 0, 0);
        const int lim = ((meta >> (4 * hf + (n >> 2))) & 1u) ? ctl[hf] - tok0 : 0;
        float x[4], e[4];
#pragma unroll
        for (int r = 0; r < 4; ++r) x[r] = (r < lim) ? s[r] * SCL : -INFINITY;
        const float tm = xrow_max(fmaxf(fmaxf(x[0], x[1]), fmaxf(x[2], x[3])));
        const float mn = fmaxf(m[hf], tm);
        const float al = __builtin_amdgcn_exp2f(m[hf] - mn);
#pragma unroll
        for (int r = 0; r < 4; ++r) e[r] = __builtin_amdgcn_exp2f(x[r] - mn);
        l[hf] = l[hf] * al + ((e[0] + e[1]) + (e[2] + e[3]));
        m[hf] = mn;
#pragma unroll
        for (int j = 0; j < 8; ++j) o[hf][j] *= al;
        pp[hf] = u32x2{pack_bf16(e[0], e[1]), pack_bf16(e[2], e[3])};
      }
    }
    if (i + 1 >= nmine) {
#pragma unroll
      for (int hf = 0; hf < 2; ++hf) {
        const float lt = xrow_sum(l[hf]);
        if (g == 0) lds.ml[opq(wave)][hf * 16 + n] = f32x2{m[hf], lt};
      }
      asm volatile("s_waitcnt lgkmcnt(0)" ::: "memory");
      if (lane == 0) __hip_atomic_fetch_add(&lds.done, 1u, WG_RELAXED);
    }
    __builtin_amdgcn_sched_barrier(0);
    u32x2 vt[8];
#pragma unroll
    for (int w = 0; w < 4; ++w) {
      vt[2 * w] = u32x2{__builtin_amdgcn_perm(vr[1][w], vr[0][w], 0x05040100u), __builtin_amdgcn_perm(vr[3][w], vr[2][w], 0x05040100u)};
      vt[2 * w + 1] = u32x2{__builtin_amdgcn_perm(vr[1][w], vr[0][w], 0x07060302u), __builtin_amdgcn_perm(vr[3][w], vr[2][w], 0x07060302u)};
    }
#pragma unroll
    for (int hf = 0; hf < 2; ++hf) {
      if ((meta >> (4 * hf)) & 0xFu) {
#pragma unroll
        for (int j = 0; j < 8; ++j)
          o[hf][j] = __builtin_amdgcn_mfma_f32_16x16x16bf16_1k(__builtin_bit_cast(s16x4, vt[j]), __builtin_bit_cast(s16x4, pp[hf]), o[hf][j], 0, 0, 0);
      }
    }
    __builtin_amdgcn_sched_barrier(0);
  } while (++i < nmine);

#pragma unroll
  for (int hf = 0; hf < 2; ++hf) {
    const int row = hf * 16 + n;
    if ((uall >> (row >> 2)) & 1u) {
#pragma unroll
      for (int j = 0; j < 8; ++j) *reinterpret_cast<f32x4*>(&lds.u.o[wave][row][16 * j + 4 * g]) = o[hf][j];
    }
  }
  __builtin_amdgcn_s_waitcnt(0x0F70);
  asm volatile("" ::"v"(cpf), "v"(pf[0]), "v"(pf[1]), "v"(pf[2]), "v"(pf[3]), "v"(pf[4]), "v"(pf[5]));

  const int kstep = nseq > 2 ? 2 : 1, kfirst = wave >> 2;
  if (kfirst < kstep) {
    while (__builtin_amdgcn_readfirstlane(__hip_atomic_load(&lds.done, WG_RELAXED)) < NW) __builtin_amdgcn_s_sleep(1);
    asm volatile("" ::: "memory");
    const f32x2 ml = lds.ml[lane & 7][rowv & 31];
    const float M = max8(ml[0]);
    const float wt = __builtin_amdgcn_exp2f(ml[0] - M);
    const float L = sum8(wt * ml[1]);
    const int wn = (int)__float_as_uint(wt * __builtin_amdgcn_rcpf(L));
    const int lsev = (int)__float_as_uint(M + __builtin_amdgcn_logf(L));
    for (int k = kfirst;; k += kstep) {
      const int row = __builtin_amdgcn_readlane(rowv, 8 * k) & 31;
      f32x2 wp[4];
#pragma unroll
      for (int w = 0; w < NW; ++w) wp[w >> 1][w & 1] = __uint_as_float((unsigned)__builtin_amdgcn_readlane(wn, 8 * k + w));
      const unsigned lh = (unsigned)__builtin_amdgcn_readlane(lsev, 8 * k) >> (16 * (lane & 1));
      unsigned tw0 = tg8 | (lh & 0xFFu), tw1 = tg8 | ((lh >> 8) & 0xFFu);
      int e0 = (row * OROW + 2 * lane) * 4, e1 = e0 + 16 * ROWS * OROW;
      unsigned pso = WS_PO + (unsigned)((h * ROWS + row) * SLOTS + sp) * 1024u;
      asm volatile("" : "+v"(tw0), "+v"(tw1), "+v"(e0), "+v"(e1), "+s"(pso), "+v"(wp[0]), "+v"(wp[1]), "+v"(wp[2]), "+v"(wp[3]));
      if (k == kfirst) lds_barrier();
      if (k >= nseq) break;
      const char* img = reinterpret_cast<const char*>(&lds.u.o[0][0][0]);
      f32x2 ov[NW];
#pragma unroll
      for (int w = 0; w < NW; ++w) ov[w] = *reinterpret_cast<const f32x2*>(img + (w < 4 ? e0 : e1) + (w & 3) * (ROWS * OROW * 4));
      f32x2 a0 = wp[0][0] * ov[0], a1 = wp[2][0] * ov[4];
#pragma unroll
      for (int w = 1; w < 4; ++w) {
        a0 += wp[w >> 1][w & 1] * ov[w];
        a1 += wp[2 + (w >> 1)][w & 1] * ov[4 + w];
      }
      a0 += a1;
      __builtin_amdgcn_raw_buffer_store_b128(u32x4{__float_as_uint(a0[0]), tw0, __float_as_uint(a0[1]), tw1}, rws, lane * 16, pso, AUX_SC1);
      if (k + kstep >= nseq) break;
    }
  } else {
    lds_barrier();
  }
  __builtin_amdgcn_sched_barrier(0);

  if (bid == 0 && wave == NW - 2) {
    for (unsigned spins = 0;; ++spins) {
      const u32x4 hd = __builtin_amdgcn_raw_buffer_load_b128(rws, lane * 16, WS_HDR, AUX_SC1);
      const bool ok = hd[0] == tag && hd[1] == tag && hd[2] == tag && hd[3] == tag;
      if (__builtin_amdgcn_ballot_w64(!ok) == 0ull || spins > SPIN_CAP) break;
      __builtin_amdgcn_s_sleep(2);
      asm volatile("" ::: "memory");
    }
    if (lane == 0) __hip_atomic_store((gu32*)(ws + WS_EPOCH), tag, DEV_RELAXED);
  }

  u32x4 og[2];
  auto gather_init = [&](int k0) {
#pragma unroll
    for (int t = 0; t < 2; ++t) {
      const unsigned tw = (k0 + 8 * t + (lane >> 3) < nsrc) ? ~tg8 : tg8;
      og[t] = u32x4{0u, tw | ((lane & 1) ? 0x80u : 0u), 0u, tw | ((lane & 1) ? 0xFFu : 0u)};
    }
  };
  gather_init(0);
  unsigned psel = (lane & 1) ? 0x04000c0cu : 0x0c0c0400u;
  const unsigned oo = (32u * ((lane >> 1) & 3) + 16u * (lane & 1) + 8u * (lane >> 5) + wave) * 2u;
  unsigned long long op = arg(5) + (unsigned)(((ix >> 2) * H + hx * G + (ix & 3)) * D * 2) + oo;
  unsigned gso = WS_PO + wave * 128u;
  asm volatile("" : "+v"(psel), "+v"(op), "+v"(og[0]), "+v"(og[1]), "+s"(gso));
  __builtin_amdgcn_sched_barrier(0);
  if (!guest && ((need >> sp) & 1u)) __builtin_amdgcn_s_sleep(10);
  while ((int)(now_ns() - tpoll) < 0) __builtin_amdgcn_s_sleep(1);
#pragma unroll
  for (int t = 0; t < 2; ++t)
    if (8 * t + (lane >> 3) < nsrc) og[t] = __builtin_amdgcn_raw_buffer_load_b128(rws, go[t], gso, AUX_SC1);
  float Mr, Wr;
  f32x2 ar;
  auto batch = [&](bool first) {
    auto stale = [&](const u32x4& v) { return (v[1] ^ tg8) | (v[3] ^ tg8); };
    if (__builtin_expect(__builtin_amdgcn_ballot_w64((stale(og[0]) | stale(og[1])) > 0xFFu) != 0ull, 0)) {
      unsigned spins = 0;
      do {
        __builtin_amdgcn_s_sleep(3);
        asm volatile("" ::: "memory");
        if (stale(og[0]) > 0xFFu) og[0] = __builtin_amdgcn_raw_buffer_load_b128(rws, go[0], gso, AUX_SC1);
        if (stale(og[1]) > 0xFFu) og[1] = __builtin_amdgcn_raw_buffer_load_b128(rws, go[1], gso, AUX_SC1);
      } while (__builtin_amdgcn_ballot_w64((stale(og[0]) | stale(og[1])) > 0xFFu) != 0ull && ++spins < SPIN_CAP);
    }
    const unsigned h0 = __builtin_amdgcn_perm(og[0][3], og[0][1], psel), h1 = __builtin_amdgcn_perm(og[1][3], og[1][1], psel);
    const float l0 = __uint_as_float(h0 | dpp<XOR1>(h0)), l1 = __uint_as_float(h1 | dpp<XOR1>(h1));
    const float M = group_max(l0, first ? l1 : fmaxf(l1, Mr));
    const float w0 = __builtin_amdgcn_exp2f(l0 - M), w1 = __builtin_amdgcn_exp2f(l1 - M);
    const float wsum = w0 + w1;
    const f32x2 part = {fmaf(w0, __uint_as_float(og[0][0]), w1 * __uint_as_float(og[1][0])), fmaf(w0, __uint_as_float(og[0][2]), w1 * __uint_as_float(og[1][2]))};
    if (first) {
      Wr = wsum;
      ar = part;
    } else {
      const float wr = __builtin_amdgcn_exp2f(Mr - M);
      Wr = Wr * wr + wsum;
      ar = ar * wr + part;
    }
    Mr = M;
  };
  batch(true);
  for (int k0 = 16; __builtin_expect(k0 < nsrc, 0); k0 += 16) {
    gather_init(k0);
#pragma unroll
    for (int t = 0; t < 2; ++t) go[t] = slot_off(k0 + 8 * t, lane, row_base());
    batch(false);
  }
  const auto sw = __builtin_amdgcn_permlane32_swap(__float_as_uint(ar[0]), __float_as_uint(ar[1]), false, false);
  const auto ww = __builtin_amdgcn_permlane32_swap(__float_as_uint(Wr), __float_as_uint(Wr), false, false);
  const float z0 = __uint_as_float(sw[0]) + __uint_as_float(sw[1]), w2 = __uint_as_float(ww[0]) + __uint_as_float(ww[1]);
  const auto s16 = __builtin_amdgcn_permlane16_swap(__float_as_uint(z0), __float_as_uint(w2), false, false);
  float y = __uint_as_float(s16[0]) + __uint_as_float(s16[1]);
  y += dppf<ROR8>(y);
  const auto f16 = __builtin_amdgcn_permlane16_swap(__float_as_uint(y), __float_as_uint(y), false, false);
  const unsigned pk = pack_bf16(__uint_as_float(f16[0]) * __builtin_amdgcn_rcpf(__uint_as_float(f16[1])), 0.f);
  if (__builtin_amdgcn_inverse_ballot_w64(0x000000FF000000FFull)) *(__attribute__((address_space(1))) unsigned short*)op = (unsigned short)pk;
}

}

extern "C" void kernel_launch(void* const* d_in, const int* in_sizes, int n_in, void* d_out, int out_size, void* d_ws,
                              size_t ws_size, hipStream_t stream) {
  const unsigned cache_bytes = (unsigned)in_sizes[1] * 2u;
  attn_paged<<<NWG, NT, 0, stream>>>(d_in[0], d_in[1], d_in[2], d_in[3], d_in[4], d_out, d_ws, cache_bytes, 0u);
}
```

```cpp
#include <hip/hip_runtime.h>
#include <hip/hip_bf16.h>
#include <cstdint>

namespace {

constexpr int B = 8, H = 32, HKV = 8, G = H / HKV, D = 128, BS = 16, NB = 512, LK = NB * BS;
constexpr int NWG = 256, NW = 8, NT = NW * 64;
constexpr int ROWS = B * G;
constexpr int PAGE_BYTES = BS * HKV * D * 2;
constexpr int TOK_BYTES = HKV * D * 2;
constexpr float SCL = 0.08838834764831845f * 1.4426950408889634f;
constexpr float MINIT = -1e30f;

constexpr int GUESTS = 2;
constexpr int NSF = 32 - GUESTS, NSS = 32 + 3 * GUESTS;
constexpr int SLOTS = 48;
constexpr int MAX_ITEMS = 24;
constexpr int WARM = 2;
constexpr int CODE_LINES = 223;

constexpr float PACE = 66.5f;
constexpr int T0_NS = 2000;
constexpr int B2_NS = 420;
constexpr float EARLY_NS = 290.f, GUEST_NS = 300.f;
constexpr int SPRINT_NS = 170;
constexpr int POLL_NS = 650;
constexpr float FA = 0.23f, FB = 0.36f;
constexpr int TP_NS = 300, TC_NS = 1300;
constexpr float E0_NS = 800.f, GX1 = 5.f, XB = 450.f, GX2 = 2.5f, XMAX = 2300.f;
constexpr int LEAD = 4, TOUCH_AT = 3, REF_A = 14, REF_B = 23;
constexpr unsigned SPIN_CAP = 1u << 20;

constexpr unsigned WS_EPOCH = 0;
constexpr unsigned WS_HDR = 4096;
constexpr unsigned WS_PROBE = 8192;
constexpr unsigned WS_PO = WS_PROBE + 128 * 128;
constexpr unsigned WS_END = WS_PO + HKV * ROWS * SLOTS * 1024;

constexpr int AUX_NT = 2, AUX_SC1 = 16;
constexpr unsigned OOB = 0x80000000u;

using u32x4 = __attribute__((ext_vector_type(4))) unsigned;
using u32x16 = __attribute__((ext_vector_type(16))) unsigned;
using i32x8 = __attribute__((ext_vector_type(8))) int;
using u32x2 = __attribute__((ext_vector_type(2))) unsigned;
using f32x4 = __attribute__((ext_vector_type(4))) float;
using f32x2 = __attribute__((ext_vector_type(2))) float;
using bf16x8 = __attribute__((ext_vector_type(8))) __bf16;
using bf16x2 = __attribute__((ext_vector_type(2))) __bf16;
using s16x4 = __attribute__((ext_vector_type(4))) short;
typedef __attribute__((address_space(1))) unsigned gu32;
typedef __attribute__((address_space(1))) char gchar;
typedef __attribute__((address_space(1))) u32x4 gu32x4;

constexpr int OROW = D + 4, BTW = 68, QROW = 17;
struct Lds {
  union {
    struct {
      u32x4 q[ROWS][QROW];
      int bt[B][NW][BTW];
    } pro;
    float o[NW][ROWS][OROW];
  } u;
  f32x2 ml[NW][ROWS];
  u32x2 items[NW][MAX_ITEMS];
  u32x4 prod[NW];
  u32x2 own[NW];
  unsigned tag;
  unsigned done;
  unsigned ready;
  unsigned lat;
  int ext;
  unsigned claim;
  unsigned pad[2];
};
constexpr int EXT_NONE = -0x40000000;

#define WG_RELAXED __ATOMIC_RELAXED, __HIP_MEMORY_SCOPE_WORKGROUP
#define DEV_RELAXED __ATOMIC_RELAXED, __HIP_MEMORY_SCOPE_AGENT

__device__ __forceinline__ __amdgpu_buffer_rsrc_t make_rsrc(const void* p, unsigned bytes) {
  return __builtin_amdgcn_make_buffer_rsrc((void*)p, 0, bytes, 0x00020000);
}
__device__ __forceinline__ void lds_barrier() { asm volatile("s_waitcnt lgkmcnt(0)\n\ts_barrier" ::: "memory"); }
__device__ __forceinline__ unsigned now_ns() { return (unsigned)__builtin_amdgcn_s_memrealtime() * 10u; }
__device__ __forceinline__ bool loads_pending() { return (__builtin_amdgcn_s_getreg(7 | (3 << 11)) & 15) != 0; }

template <int CTRL>
__device__ __forceinline__ unsigned dpp(unsigned x) {
  return (unsigned)__builtin_amdgcn_mov_dpp((int)x, CTRL, 0xf, 0xf, true);
}
template <int CTRL>
__device__ __forceinline__ float dppf(float x) {
  return __uint_as_float(dpp<CTRL>(__float_as_uint(x)));
}
constexpr int XOR1 = 0xB1, XOR2 = 0x4E, HMIR = 0x141, ROR8 = 0x128;

__device__ __forceinline__ float xrow_max(float x) {
  auto s = __builtin_amdgcn_permlane16_swap(__float_as_uint(x), __float_as_uint(x), false, false);
  x = fmaxf(__uint_as_float(s[0]), __uint_as_float(s[1]));
  auto t = __builtin_amdgcn_permlane32_swap(__float_as_uint(x), __float_as_uint(x), false, false);
  return fmaxf(__uint_as_float(t[0]), __uint_as_float(t[1]));
}
__device__ __forceinline__ float xrow_sum(float x) {
  auto s = __builtin_amdgcn_permlane16_swap(__float_as_uint(x), __float_as_uint(x), false, false);
  x = __uint_as_float(s[0]) + __uint_as_float(s[1]);
  auto t = __builtin_amdgcn_permlane32_swap(__float_as_uint(x), __float_as_uint(x), false, false);
  return __uint_as_float(t[0]) + __uint_as_float(t[1]);
}
__device__ __forceinline__ float group_max(float a, float b) {
  float r, t;
  asm volatile(
      "v_max_f32 %0, %2, %3\n\ts_nop 1\n\t"
      "v_max_f32_dpp %0, %0, %0 row_ror:8 row_mask:0xf bank_mask:0xf\n\t"
      "v_mov_b32 %1, %0\n\ts_nop 1\n\t"
      "v_permlane16_swap_b32 %0, %1\n\t"
      "v_max_f32 %0, %0, %1\n\t"
      "v_mov_b32 %1, %0\n\ts_nop 1\n\t"
      "v_permlane32_swap_b32 %0, %1\n\t"
      "v_max_f32 %0, %0, %1"
      : "=&v"(r), "=&v"(t)
      : "v"(a), "v"(b));
  return r;
}
__device__ __forceinline__ float max8(float x) {
  x = fmaxf(x, dppf<XOR1>(x));
  x = fmaxf(x, dppf<XOR2>(x));
  return fmaxf(x, dppf<HMIR>(x));
}
__device__ __forceinline__ float sum8(float x) {
  x += dppf<XOR1>(x);
  x += dppf<XOR2>(x);
  return x + dppf<HMIR>(x);
}
__device__ __forceinline__ float sum16(float x) {
  x = sum8(x);
  return x + dppf<ROR8>(x);
}
__device__ __forceinline__ unsigned or8(unsigned x) {
  x |= dpp<XOR1>(x);
  x |= dpp<XOR2>(x);
  return x | dpp<HMIR>(x);
}
__device__ __forceinline__ unsigned wave_or(unsigned x) {
  x = or8(x);
  x |= dpp<ROR8>(x);
  auto s = __builtin_amdgcn_permlane16_swap(x, x, false, false);
  x = s[0] | s[1];
  auto t = __builtin_amdgcn_permlane32_swap(x, x, false, false);
  return t[0] | t[1];
}
__device__ __forceinline__ unsigned long long or8_u64(unsigned lo, unsigned hi) {
  return (unsigned)__builtin_amdgcn_readfirstlane(or8(lo)) | ((unsigned long long)(unsigned)__builtin_amdgcn_readfirstlane(or8(hi)) << 32);
}
__device__ __forceinline__ unsigned pack_bf16(float lo, float hi) {
  return __builtin_bit_cast(unsigned, __builtin_convertvector(f32x2{lo, hi}, bf16x2));
}
__device__ __forceinline__ int mbcnt64(unsigned long long m, unsigned z = 0u) {
  return (int)__builtin_amdgcn_mbcnt_hi((unsigned)(m >> 32), __builtin_amdgcn_mbcnt_lo((unsigned)m, z));
}

#define XCHG_BODY(QP)                                                                 \
  "v_cndmask_b32_dpp %0, %12, %8, vcc " QP " row_mask:0xf bank_mask:0xf\n\t"          \
  "v_cndmask_b32_dpp %1, %13, %9, vcc " QP " row_mask:0xf bank_mask:0xf\n\t"          \
  "v_cndmask_b32_dpp %2, %14, %10, vcc " QP " row_mask:0xf bank_mask:0xf\n\t"         \
  "v_cndmask_b32_dpp %3, %15, %11, vcc " QP " row_mask:0xf bank_mask:0xf\n\t"         \
  "s_not_b64 vcc, vcc\n\t"                                                            \
  "v_cndmask_b32_dpp %4, %8, %12, vcc " QP " row_mask:0xf bank_mask:0xf\n\t"          \
  "v_cndmask_b32_dpp %5, %9, %13, vcc " QP " row_mask:0xf bank_mask:0xf\n\t"          \
  "v_cndmask_b32_dpp %6, %10, %14, vcc " QP " row_mask:0xf bank_mask:0xf\n\t"         \
  "v_cndmask_b32_dpp %7, %11, %15, vcc " QP " row_mask:0xf bank_mask:0xf"
#define XCHG_OPS                                                                              \
  : "=&v"(x0), "=&v"(x1), "=&v"(x2), "=&v"(x3), "=&v"(y0), "=&v"(y1), "=&v"(y2), "=&v"(y3)    \
  : "v"(a[0]), "v"(a[1]), "v"(a[2]), "v"(a[3]), "v"(b[0]), "v"(b[1]), "v"(b[2]), "v"(b[3])    \
  : "vcc", "scc"
template <int BIT>
__device__ __forceinline__ void xchg(const u32x4& a, const u32x4& b, u32x4& x, u32x4& y) {
  unsigned x0, x1, x2, x3, y0, y1, y2, y3;
  if constexpr (BIT == 0)
    asm volatile("s_mov_b32 vcc_lo, 0x55555555\n\ts_mov_b32 vcc_hi, 0x55555555\n\t" XCHG_BODY("quad_perm:[1,0,3,2]") XCHG_OPS);
  else
    asm volatile("s_mov_b32 vcc_lo, 0x33333333\n\ts_mov_b32 vcc_hi, 0x33333333\n\t" XCHG_BODY("quad_perm:[2,3,0,1]") "\n\ts_nop 1" XCHG_OPS);
  x = u32x4{x0, x1, x2, x3};
  y = u32x4{y0, y1, y2, y3};
}

__global__ void __launch_bounds__(NT, 2)
attn_paged(const void* q_arg, const void* k_arg, const void* v_arg, const void* bt_arg, const void* ctx_arg, void* out_arg,
           void* ws_arg, unsigned cache_bytes_arg, unsigned zero_arg) {
  __shared__ __attribute__((aligned(16))) Lds lds;
  u32x16 ka;
  const auto kp = __builtin_amdgcn_kernarg_segment_ptr();
  asm volatile("s_load_dwordx16 %0, %1, 0x0" : "=&s"(ka) : "s"(kp));
  const int tid = threadIdx.x, lane = tid & 63;
  const int wave = __builtin_amdgcn_readfirstlane(tid >> 6);
  const int n = lane & 15, g = lane >> 4;
  const int bid = blockIdx.x;
  const int hx = bid & 7, ix = bid >> 3;
  const int col = NW * lane + wave;

  unsigned cpf;
  {
    unsigned long long pc;
    asm volatile("s_getpc_b64 %0" : "=s"(pc));
    cpf = *(const gu32*)((pc & ~63ull) + (tid < CODE_LINES ? tid : 0) * 64u);
  }
  unsigned bto = (unsigned)(wave * NB * 4 + lane * 16);
  unsigned wvo = (unsigned)((lane >> 5) * PAGE_BYTES + ((lane & 31) >> 1) * TOK_BYTES + (lane & 1) * 128) +
                 (unsigned)(ix * 3 * WARM) * (unsigned)PAGE_BYTES + (unsigned)(hx * D * 2);
  unsigned tvk = ((unsigned)(ix + 32 * lane) << 21) + (unsigned)bid * 4352u;
  unsigned tvw = WS_PO + (unsigned)((((hx & 4) | (lane & 3)) * ROWS + ix) * SLOTS + SLOTS - 1) * 1024u;
  unsigned tvo = (unsigned)(((ix >> 2) * H + hx * G + (ix & 3)) * D * 2);
  tvk = lane < 2 ? tvk : OOB;
  tvw = lane < 4 ? tvw : OOB;
  tvo = lane < 1 ? tvo : OOB;
  asm volatile("" : "+v"(wvo), "+v"(tvk), "+v"(tvw), "+v"(tvo));
  tvk = tid >= 448 ? wvo : tvk;
  asm volatile("" : "+v"(tvk));
  unsigned xv0 = tid >= 384 ? tvk : tvw, xv1 = tid >= 384 ? tvk : tvo;
  asm volatile("s_waitcnt lgkmcnt(0)" : "+s"(ka), "+v"(bto), "+v"(xv0), "+v"(xv1) : "s"(kp));
  auto arg = [&](int k) { return (unsigned long long)ka[2 * k] | ((unsigned long long)ka[2 * k + 1] << 32); };
  const gchar* const q = (const gchar*)arg(0);
  const char* const kc = (const char*)(const gchar*)arg(1);
  const char* const vc = (const char*)(const gchar*)arg(2);
  const gchar* const bt = (const gchar*)arg(3);
  const char* const out = (const char*)(const gchar*)arg(5);
  gchar* const ws = (gchar*)arg(6);
  const unsigned cache_bytes = ka[14];

  const u32x4 bt0 = *(const gu32x4*)(bt + bto);
  const u32x4 bt1 = *(const gu32x4*)(bt + bto + 1024);
  const i32x8 cv = *(const __attribute__((address_space(4))) i32x8*)arg(4);
  unsigned long long t64 = __builtin_amdgcn_s_memrealtime();
  int cl[B];
#pragma unroll
  for (int b = 0; b < B; ++b) cl[b] = cv[b];
  __builtin_amdgcn_sched_barrier(0);
  asm volatile("s_barrier" ::: "memory");

  unsigned pf[6];
  auto warm = [&](int slot) {
    const unsigned nrec = wave == NW - 1 ? cache_bytes : 0u;
    pf[2 * slot] = __builtin_amdgcn_raw_buffer_load_b32(make_rsrc(kc, nrec), wvo, slot * WARM * PAGE_BYTES, 0);
    pf[2 * slot + 1] = __builtin_amdgcn_raw_buffer_load_b32(make_rsrc(vc, nrec), wvo, slot * WARM * PAGE_BYTES, 0);
    __builtin_amdgcn_sched_barrier(0);
  };
  static_assert(WARM == 2, "one load instruction covers two pages");
  {
    const bool aux = wave == NW - 3;
    const unsigned nrec = wave >= NW - 2 ? cache_bytes : 0u;
    pf[0] = __builtin_amdgcn_raw_buffer_load_b32(make_rsrc(aux ? (const char*)ws : kc, aux ? WS_END : nrec), xv0, 0, 0);
    pf[1] = __builtin_amdgcn_raw_buffer_load_b32(make_rsrc(aux ? out : vc, aux ? B * H * D * 2u : nrec), xv1, 0, 0);
    __builtin_amdgcn_sched_barrier(0);
  }
  const int hs = (hx & 4) | ((1 - (int)(ka[2] >> 8)) & 3);
  const bool guest = hx != hs && ix >= NSF;
  int h = guest ? hs : hx;
  const int spg = ((hx - hs - 1) & 3) * GUESTS - NSF, spr = hx == hs ? 3 * GUESTS : 0;
  int sp = ix + (guest ? spg : spr);
  asm volatile("" : "+s"(h), "+s"(sp));
  u32x4 qv = *(const gu32x4*)(q + (unsigned)((wave * H + h * G) * D * 2) + lane * 16u);
  const unsigned epoch = __hip_atomic_load((gu32*)(ws + WS_EPOCH), DEV_RELAXED);
  __builtin_amdgcn_sched_barrier(0);
  unsigned koff[2], voff[2];
#pragma unroll
  for (int t = 0; t < 2; ++t) {
    koff[t] = (unsigned)((4 * (n >> 2) + 2 * t) * TOK_BYTES + g * 64 + (n & 3) * 16);
    voff[t] = (unsigned)((4 * g + 2 * t) * TOK_BYTES + n * 16);
  }
  f32x4 o[2][8];
  float m[2], l[2];
#pragma unroll
  for (int hf = 0; hf < 2; ++hf) {
    m[hf] = MINIT;
    l[hf] = 0.f;
#pragma unroll
    for (int j = 0; j < 8; ++j) o[hf][j] = f32x4{0.f, 0.f, 0.f, 0.f};
    asm volatile("" : "+v"(o[hf][0]), "+v"(o[hf][1]), "+v"(o[hf][2]), "+v"(o[hf][3]), "+v"(o[hf][4]), "+v"(o[hf][5]), "+v"(o[hf][6]), "+v"(o[hf][7]),
                 "+v"(m[hf]), "+v"(l[hf]), "+v"(koff[hf]), "+v"(voff[hf]));
  }
  __builtin_amdgcn_sched_barrier(0);
  asm volatile("" : "+s"(t64) : "s"(cl[0]));
  const unsigned tstart = (unsigned)t64;
  __builtin_amdgcn_sched_barrier(0);
  warm(1);
  asm volatile("" : "+s"(cl[0]), "+s"(cl[1]), "+s"(cl[2]), "+s"(cl[3]), "+s"(cl[4]), "+s"(cl[5]), "+s"(cl[6]), "+s"(cl[7]));

  int cw = cl[0];
#pragma unroll
  for (int b = 1; b < B; ++b) cw = (wave == b) ? cl[b] : cw;
  unsigned long long live[B];
  unsigned lvb = 0;
#pragma unroll
  for (int b = 0; b < B; ++b) {
    cl[b] = (cl[b] <= 0 || cl[b] > LK) ? LK : cl[b];
    live[b] = __builtin_amdgcn_ballot_w64(col * BS < cl[b]);
    lvb |= (col * BS < cl[b]) ? (1u << b) : 0u;
  }
  int ctl[2];
#pragma unroll
  for (int hf = 0; hf < 2; ++hf) {
    int c = cl[4 * hf];
#pragma unroll
    for (int a = 1; a < 4; ++a) c = ((n >> 2) == a) ? cl[4 * hf + a] : c;
    ctl[hf] = c - 4 * g;
  }
  asm volatile("" : "+v"(lvb), "+v"(ctl[0]), "+v"(ctl[1]));
#pragma unroll
  for (int e = 0; e < 4; ++e) {
    lds.u.pro.bt[wave][4 * (lane & 1) + e][lane >> 1] = (int)bt0[e];
    lds.u.pro.bt[wave][4 * (lane & 1) + e][32 + (lane >> 1)] = (int)bt1[e];
  }
  lds_barrier();
  warm(2);

  int pv[B];
#pragma unroll
  for (int b = 0; b < B; ++b) pv[b] = lds.u.pro.bt[b][wave][lane];
  unsigned long long lead[B], heavy[B];
  unsigned eqb = 0;
  {
    unsigned long long eq[B];
#pragma unroll
    for (int b = 0; b < B; ++b) {
      pv[b] = (col * BS < cl[b]) ? pv[b] : (b == 0 ? (int)0x80000000 : pv[b - 1]);
      eq[b] = b == 0 ? 0ull : __builtin_amdgcn_ballot_w64(pv[b] == pv[b - 1]);
      if (b) eqb |= (pv[b] == pv[b - 1]) ? (1u << b) : 0u;
    }
    unsigned long long fol = 0;
#pragma unroll
    for (int b = B - 1; b >= 0; --b) {
      lead[b] = live[b] & ~eq[b];
      heavy[b] = lead[b] & fol;
      fol = eq[b] & (live[b] | fol);
    }
  }
  asm volatile("" : "+v"(eqb));
  auto members = [&](int b) { return (((2u << __builtin_ctz(~(eqb >> (b + 1)))) - 1u) << b) & lvb; };
  int base[B + 1], cnt = 0;
  base[0] = 0;
#pragma unroll
  for (int b = 0; b < B; ++b) {
    const int c = __builtin_popcountll(lead[b]);
    cnt += c;
    base[b + 1] = base[b] + 8 * c + __builtin_popcountll(heavy[b]);
  }
  const int total = base[B];
  const unsigned ta = (unsigned)(total * sp);
  const unsigned mg = h == hs ? 0xFFFFFFFFu / NSS + 1u : 0xFFFFFFFFu / NSF + 1u;
  const int lo = (int)__umulhi(ta, mg) + wave, hi = (int)__umulhi(ta + (unsigned)total, mg) + wave;
  int nmine = 0;
#pragma unroll
  for (int b = 0; b < B; ++b) {
    if (base[b] + 7 < hi && base[b + 1] > lo) {
      const int pos = base[b] + 7 - lo + 8 * mbcnt64(lead[b]) + mbcnt64(heavy[b]);
      const unsigned long long sel = lead[b] & __builtin_amdgcn_ballot_w64((unsigned)pos < (unsigned)(hi - lo));
      if (__builtin_amdgcn_inverse_ballot_w64(sel)) lds.items[wave][nmine + mbcnt64(sel)] = u32x2{(unsigned)pv[b], members(b) | ((unsigned)col << 8)};
      nmine += __builtin_popcountll(sel);
    }
  }
  __builtin_amdgcn_fence(__ATOMIC_RELEASE, "wavefront");
  __builtin_amdgcn_wave_barrier();
  __builtin_amdgcn_fence(__ATOMIC_ACQUIRE, "wavefront");
  u32x2 myit = lds.items[wave][lane < MAX_ITEMS ? lane : 0];

  const unsigned hoff = (unsigned)(h * D * 2);
  u32x4 kr[4], vr[4];
  auto issue = [&](u32x4 (&r)[4], const char* src, const unsigned (&off)[2], int i) {
    const unsigned phys = (unsigned)__builtin_amdgcn_readlane((int)myit[0], i);
    const __amdgpu_buffer_rsrc_t rs = make_rsrc(src, i < nmine ? cache_bytes : 0u);
    const unsigned so = phys * (unsigned)PAGE_BYTES + hoff;
#pragma unroll
    for (int t = 0; t < 4; ++t) r[t] = __builtin_amdgcn_raw_buffer_load_b128(rs, off[t >> 1] + TOK_BYTES * (t & 1), so, AUX_NT);
  };
  issue(kr, kc, koff, 0);
  __builtin_amdgcn_sched_barrier(0);

  myit[1] = lane < nmine ? myit[1] : 0u;
  {
    const unsigned um = wave_or(myit[1] & 0xFFu);
    if (lane == 0) lds.own[wave] = u32x2{um, (unsigned)cnt};
  }
  if (cw <= 0) qv = u32x4{0u, 0u, 0u, 0u};
  lds.u.pro.q[wave * G + g][n] = qv;
  if (wave == 0 && lane == 0) {
    lds.tag = epoch + 1u;
    lds.done = 0u;
    lds.ready = 0u;
    lds.lat = 0u;
    lds.ext = EXT_NONE;
    lds.claim = 0u;
  }
  lds_barrier();
  const unsigned tready = now_ns();
  u32x4 qf[2][4];
#pragma unroll
  for (int hf = 0; hf < 2; ++hf)
#pragma unroll
    for (int s = 0; s < 4; ++s) qf[hf][s] = lds.u.pro.q[hf * 16 + n][4 * g + s];
  const u32x2 ownv = lds.own[lane & 7];
  const unsigned uall = __builtin_amdgcn_readfirstlane(or8(ownv[0]));
  const int nseq = __builtin_popcount(uall);
  const unsigned tag = __builtin_amdgcn_readfirstlane(lds.tag), tg8 = tag << 8;
  const __amdgpu_buffer_rsrc_t rws = make_rsrc((const char*)ws, WS_END);
  auto opq = [&](int x) { asm volatile("" : "+s"(x)); return x; };
  auto opv = [&](int x) { asm volatile("" : "+v"(x)); return x; };

  const float span = 0.125f * PACE * sum8((float)ownv[1]);
  const float dur = fmaxf(span - EARLY_NS * (float)(nseq - 1) - (guest ? GUEST_NS : 0.f), 0.f);
  int del = __builtin_amdgcn_readfirstlane((int)(dur * __builtin_amdgcn_rcpf((float)(2 * nmine + (nmine == 0))))) & -(nmine > 0);
  const unsigned tb0 = tstart * 10u + T0_NS, tb1 = tready - B2_NS, tbase = (int)(tb1 - tb0) > 0 ? tb1 : tb0;
  unsigned due = tbase + (unsigned)(((wave - LEAD) * del) >> 3);
  unsigned tpoll = tbase + POLL_NS + (unsigned)__builtin_amdgcn_readfirstlane((int)span);
  const unsigned tma = tbase + (unsigned)__builtin_amdgcn_readfirstlane((int)(span * FA));
  const unsigned tmb = tbase + (unsigned)__builtin_amdgcn_readfirstlane((int)(span * FB));
  const bool refwg = ix == REF_A || ix == REF_B;
  auto posto = [&]() { const int b2 = opq(bid); return WS_PROBE + (unsigned)((b2 & 7) + ((b2 >> 3) == REF_B ? 8 : 0)) * 128u; };
  auto claim = [&](unsigned bit) {
    unsigned old = 0;
    if (lane == 0) old = __hip_atomic_fetch_or(&lds.claim, bit, WG_RELAXED);
    return (__builtin_amdgcn_readfirstlane(old) & bit) == 0u;
  };
  bool settled = false;
  unsigned tlast = 0, topen = 0;
  auto pace = [&](int left) {
    unsigned now = now_ns();
    if (!settled && (int)(now - tma) >= 0) {
      if (topen) {
        if (lane == 0) __hip_atomic_fetch_add(&lds.lat, min(now - topen - 120u, 2500u) + (1u << 20), WG_RELAXED);
        topen = 0;
      }
      if ((int)(now - tmb) < 0) {
        if (refwg) {
          bool pend;
          for (;;) {
            now = now_ns();
            pend = loads_pending();
            if (!pend || (int)(now - (due + del)) >= 0) break;
            __builtin_amdgcn_s_sleep(1);
          }
          if (pend) topen = tlast | 1u;
          else if (lane == 0) __hip_atomic_fetch_add(&lds.lat, min(now - tlast, 2500u) + (1u << 20), WG_RELAXED);
        }
      } else {
        if (refwg && (int)(now - (tmb + TP_NS)) >= 0 && claim(1u)) {
          const int ln = opv(lane);
          const unsigned acc = __hip_atomic_load(&lds.lat, WG_RELAXED);
          __builtin_amdgcn_raw_buffer_store_b128(u32x4{acc, tag, tbase, tag}, rws, (ln >> 3) * 2048 + (ln & 7) * 16, posto(), AUX_SC1);
        } else if ((int)(now - (tmb + TC_NS)) >= 0 && claim(2u)) {
          const int ln = opv(lane);
          const u32x4 v = __builtin_amdgcn_raw_buffer_load_b128(rws, ln < 16 ? ln * 128u : OOB, WS_PROBE + (unsigned)(opq(bid) & 7) * 2048u, AUX_SC1);
          const bool ok = ln < 16 && v[1] == tag && v[3] == tag;
          const float skew = sum16(ok ? (float)(int)(v[2] - tbase) : 0.f) * __builtin_amdgcn_rcpf(sum16(ok ? 1.f : 0.f));
          const float sum = sum16(ok ? (float)(v[0] & 0xFFFFFu) : 0.f), cn = sum16(ok ? (float)(v[0] >> 20) : 0.f);
          const float e = sum * __builtin_amdgcn_rcpf(cn) - E0_NS;
          const float x = fminf(fmaxf(fminf(e * GX1, XB + e * GX2), 0.f), XMAX) * (span * (1.f / 15200.f)) + fminf(fmaxf(skew, -900.f), 900.f);
          if (lane == 0) __hip_atomic_store(&lds.ext, cn > 0.f ? (int)x : 0, WG_RELAXED);
        }
        const int x = __builtin_amdgcn_readfirstlane(__hip_atomic_load(&lds.ext, WG_RELAXED));
        if (x != EXT_NONE) {
          settled = true;
          tpoll += (unsigned)x;
          del += __builtin_amdgcn_readfirstlane((int)((float)x * __builtin_amdgcn_rcpf((float)left)));
        }
      }
    }
    due += (unsigned)(left <= 2 ? del - SPRINT_NS : del);
    while ((int)((now = now_ns()) - due) < 0) __builtin_amdgcn_s_sleep(1);
    tlast = now;
  };

  auto producers = [&]() {
    const int b2 = opq(bid), w2 = opq(wave), tb = b2 >> 5;
    if (w2 == 0 && lane == 0) __builtin_amdgcn_raw_buffer_store_b32(tag, rws, 0, WS_HDR + (unsigned)b2 * 4u, AUX_SC1);
    int idx = 0;
    const unsigned z = (unsigned)opv(0);
#pragma unroll
    for (int a = 0; a < B; ++a) {
      const int ia = base[a] + 7 + 8 * mbcnt64(lead[a], z) + mbcnt64(heavy[a], z);
      idx = __builtin_amdgcn_inverse_ballot_w64(a <= tb ? lead[a] : 0ull) ? ia : idx;
    }
    const int s2 = (int)(((float)(((b2 & 7) == hs ? NSS : NSF) * (idx - w2 + 1) - 1) + 0.5f) * __builtin_amdgcn_rcpf((float)opq(total)));
    const unsigned bit = ((lvb >> tb) & 1u) << (s2 & 31), blo = s2 < 32 ? bit : 0u, bhi = s2 < 32 ? 0u : bit;
    const bool shr = ((eqb >> tb) & 1u) || members(tb) != (1u << tb);
    const u32x4 r4 = {wave_or(blo), wave_or(bhi), wave_or(shr ? blo : 0u), wave_or(shr ? bhi : 0u)};
    if (lane == 0) {
      lds.prod[w2] = r4;
      asm volatile("s_waitcnt lgkmcnt(0)" ::: "memory");
      __hip_atomic_fetch_add(&lds.ready, 1u, WG_RELAXED);
    }
  };
  unsigned long long need = 0;
  int nsrc = 0, srcv = 0, rowv = 0;
  unsigned go[2] = {0u, 0u};
  auto row_base = [&]() { const int b2 = opq(bid); return (unsigned)(((b2 & 7) * ROWS + (b2 >> 3)) * SLOTS); };
  auto slot_off = [&](int k0, int ln, unsigned rb) { return (rb + (unsigned)__builtin_amdgcn_ds_bpermute(4 * (k0 + (ln >> 3)), srcv)) * 1024u + (ln & 7) * 16u; };
  auto resolve = [&]() {
    while (__builtin_amdgcn_readfirstlane(__hip_atomic_load(&lds.ready, WG_RELAXED)) < NW) __builtin_amdgcn_s_sleep(1);
    asm volatile("" ::: "memory");
    const int ln = opv(lane), w2 = opq(wave);
    const unsigned rb = row_base();
    const u32x4 pr = lds.prod[ln & 7];
    need = or8_u64(pr[0], pr[1]);
    const unsigned long long early = or8_u64(pr[2], pr[3]);
    const int seqv = __builtin_amdgcn_ds_permute(ln < B && ((uall >> ln) & 1u) ? 4 * __builtin_popcount(uall & ((1u << (ln & 7)) - 1u)) : 252, ln);
    rowv = 4 * __builtin_amdgcn_ds_bpermute(4 * (ln >> 3), seqv) + (w2 & 3);
    nsrc = __builtin_popcountll(need);
    srcv = __builtin_amdgcn_ds_permute(__builtin_amdgcn_inverse_ballot_w64(need) ? 4 * mbcnt64(need) : 252, ln);
#pragma unroll
    for (int t = 0; t < 2; ++t) go[t] = slot_off(8 * t, ln, rb);
    return __builtin_amdgcn_inverse_ballot_w64(need & ~early) ? WS_PO + (rb + (unsigned)ln) * 1024u + w2 * 128u : WS_END;
  };
  const int ires = min(TOUCH_AT, nmine - 1);

  unsigned tch = 0;
  int i = 0;
  asm volatile("" : "+s"(i));
  do {
    pace(2 * (nmine - i) - 1);
    issue(vr, vc, voff, i);
    __builtin_amdgcn_sched_barrier(0);
    if (i == 0) producers();
    const unsigned meta = (unsigned)__builtin_amdgcn_readlane((int)myit[1], i);
    const int tok0 = (int)(meta >> 8) * BS;
    u32x4 t0, t1, t2, t3, kt[4];
    asm volatile("" ::"v"(tch));
    xchg<0>(kr[0], kr[1], t0, t1);
    xchg<0>(kr[2], kr[3], t2, t3);
    xchg<1>(t0, t2, kt[0], kt[2]);
    xchg<1>(t1, t3, kt[1], kt[3]);
    __builtin_amdgcn_sched_barrier(0);
    unsigned tcho = WS_END;
    if (i == 0 && refwg && wave == NW - 1) {
      const int ln = opv(lane);
      tcho = ln < 8 ? posto() + ln * 2048u : WS_END;
    }
    if (i >= ires && nsrc == 0) tcho = resolve();
    if (i + 1 < nmine) pace(2 * (nmine - i) - 2);
    issue(kr, kc, koff, i + 1);
    tch = __builtin_amdgcn_raw_buffer_load_b32(rws, tcho, 0, AUX_SC1);
    __builtin_amdgcn_sched_barrier(0);
    u32x2 pp[2];
#pragma unroll
    for (int hf = 0; hf < 2; ++hf) {
      if ((meta >> (4 * hf)) & 0xFu) {
        f32x4 s = {0.f, 0.f, 0.f, 0.f};
#pragma unroll
        for (int st = 0; st < 4; ++st)
          s = __builtin_amdgcn_mfma_f32_16x16x32_bf16(__builtin_bit_cast(bf16x8, kt[st]), __builtin_bit_cast(bf16x8, qf[hf][st]), s, 0, 0, 0);
        const int lim = ((meta >> (4 * hf + (n >> 2))) & 1u) ? ctl[hf] - tok0 : 0;
        float x[4], e[4];
#pragma unroll
        for (int r = 0; r < 4; ++r) x[r] = (r < lim) ? s[r] * SCL : -INFINITY;
        const float tm = xrow_max(fmaxf(fmaxf(x[0], x[1]), fmaxf(x[2], x[3])));
        const float mn = fmaxf(m[hf], tm);
        const float al = __builtin_amdgcn_exp2f(m[hf] - mn);
#pragma unroll
        for (int r = 0; r < 4; ++r) e[r] = __builtin_amdgcn_exp2f(x[r] - mn);
        l[hf] = l[hf] * al + ((e[0] + e[1]) + (e[2] + e[3]));
        m[hf] = mn;
#pragma unroll
        for (int j = 0; j < 8; ++j) o[hf][j] *= al;
        pp[hf] = u32x2{pack_bf16(e[0], e[1]), pack_bf16(e[2], e[3])};
      }
    }
    if (i + 1 >= nmine) {
#pragma unroll
      for (int hf = 0; hf < 2; ++hf) {
        const float lt = xrow_sum(l[hf]);
        if (g == 0) lds.ml[opq(wave)][hf * 16 + n] = f32x2{m[hf], lt};
      }
      asm volatile("s_waitcnt lgkmcnt(0)" ::: "memory");
      if (lane == 0) __hip_atomic_fetch_add(&lds.done, 1u, WG_RELAXED);
    }
    __builtin_amdgcn_sched_barrier(0);
    u32x2 vt[8];
#pragma unroll
    for (int w = 0; w < 4; ++w) {
      vt[2 * w] = u32x2{__builtin_amdgcn_perm(vr[1][w], vr[0][w], 0x05040100u), __builtin_amdgcn_perm(vr[3][w], vr[2][w], 0x05040100u)};
      vt[2 * w + 1] = u32x2{__builtin_amdgcn_perm(vr[1][w], vr[0][w], 0x07060302u), __builtin_amdgcn_perm(vr[3][w], vr[2][w], 0x07060302u)};
    }
#pragma unroll
    for (int hf = 0; hf < 2; ++hf) {
      if ((meta >> (4 * hf)) & 0xFu) {
#pragma unroll
        for (int j = 0; j < 8; ++j)
          o[hf][j] = __builtin_amdgcn_mfma_f32_16x16x16bf16_1k(__builtin_bit_cast(s16x4, vt[j]), __builtin_bit_cast(s16x4, pp[hf]), o[hf][j], 0, 0, 0);
      }
    }
    __builtin_amdgcn_sched_barrier(0);
  } while (++i < nmine);

#pragma unroll
  for (int hf = 0; hf < 2; ++hf) {
    const int row = hf * 16 + n;
    if ((uall >> (row >> 2)) & 1u) {
#pragma unroll
      for (int j = 0; j < 8; ++j) *reinterpret_cast<f32x4*>(&lds.u.o[wave][row][16 * j + 4 * g]) = o[hf][j];
    }
  }
  __builtin_amdgcn_s_waitcnt(0x0F70);
  asm volatile("" ::"v"(cpf), "v"(pf[0]), "v"(pf[1]), "v"(pf[2]), "v"(pf[3]), "v"(pf[4]), "v"(pf[5]));

  const int kstep = nseq > 2 ? 2 : 1, kfirst = wave >> 2;
  if (kfirst < kstep) {
    while (__builtin_amdgcn_readfirstlane(__hip_atomic_load(&lds.done, WG_RELAXED)) < NW) __builtin_amdgcn_s_sleep(1);
    asm volatile("" ::: "memory");
    const f32x2 ml = lds.ml[lane & 7][rowv & 31];
    const float M = max8(ml[0]);
    const float wt = __builtin_amdgcn_exp2f(ml[0] - M);
    const float L = sum8(wt * ml[1]);
    const int wn = (int)__float_as_uint(wt * __builtin_amdgcn_rcpf(L));
    const int lsev = (int)__float_as_uint(M + __builtin_amdgcn_logf(L));
    for (int k = kfirst;; k += kstep) {
      const int row = __builtin_amdgcn_readlane(rowv, 8 * k) & 31;
      f32x2 wp[4];
#pragma unroll
      for (int w = 0; w < NW; ++w) wp[w >> 1][w & 1] = __uint_as_float((unsigned)__builtin_amdgcn_readlane(wn, 8 * k + w));
      const unsigned lh = (unsigned)__builtin_amdgcn_readlane(lsev, 8 * k) >> (16 * (lane & 1));
      unsigned tw0 = tg8 | (lh & 0xFFu), tw1 = tg8 | ((lh >> 8) & 0xFFu);
      int e0 = (row * OROW + 2 * lane) * 4, e1 = e0 + 16 * ROWS * OROW;
      unsigned pso = WS_PO + (unsigned)((h * ROWS + row) * SLOTS + sp) * 1024u;
      asm volatile("" : "+v"(tw0), "+v"(tw1), "+v"(e0), "+v"(e1), "+s"(pso), "+v"(wp[0]), "+v"(wp[1]), "+v"(wp[2]), "+v"(wp[3]));
      if (k == kfirst) lds_barrier();
      if (k >= nseq) break;
      const char* img = reinterpret_cast<const char*>(&lds.u.o[0][0][0]);
      f32x2 ov[NW];
#pragma unroll
      for (int w = 0; w < NW; ++w) ov[w] = *reinterpret_cast<const f32x2*>(img + (w < 4 ? e0 : e1) + (w & 3) * (ROWS * OROW * 4));
      f32x2 a0 = wp[0][0] * ov[0], a1 = wp[2][0] * ov[4];
#pragma unroll
      for (int w = 1; w < 4; ++w) {
        a0 += wp[w >> 1][w & 1] * ov[w];
        a1 += wp[2 + (w >> 1)][w & 1] * ov[4 + w];
      }
      a0 += a1;
      __builtin_amdgcn_raw_buffer_store_b128(u32x4{__float_as_uint(a0[0]), tw0, __float_as_uint(a0[1]), tw1}, rws, lane * 16, pso, AUX_SC1);
      if (k + kstep >= nseq) break;
    }
  } else {
    lds_barrier();
  }
  __builtin_amdgcn_sched_barrier(0);

  if (bid == 0 && wave == NW - 2) {
    for (unsigned spins = 0;; ++spins) {
      const u32x4 hd = __builtin_amdgcn_raw_buffer_load_b128(rws, lane * 16, WS_HDR, AUX_SC1);
      const bool ok = hd[0] == tag && hd[1] == tag && hd[2] == tag && hd[3] == tag;
      if (__builtin_amdgcn_ballot_w64(!ok) == 0ull || spins > SPIN_CAP) break;
      __builtin_amdgcn_s_sleep(2);
      asm volatile("" ::: "memory");
    }
    if (lane == 0) __hip_atomic_store((gu32*)(ws + WS_EPOCH), tag, DEV_RELAXED);
  }

  u32x4 og[2];
  auto gather_init = [&](int k0) {
#pragma unroll
    for (int t = 0; t < 2; ++t) {
      const unsigned tw = (k0 + 8 * t + (lane >> 3) < nsrc) ? ~tg8 : tg8;
      og[t] = u32x4{0u, tw | ((lane & 1) ? 0x80u : 0u), 0u, tw | ((lane & 1) ? 0xFFu : 0u)};
    }
  };
  gather_init(0);
  unsigned psel = (lane & 1) ? 0x04000c0cu : 0x0c0c0400u;
  const unsigned oo = (32u * ((lane >> 1) & 3) + 16u * (lane & 1) + 8u * (lane >> 5) + wave) * 2u;
  unsigned long long op = arg(5) + (unsigned)(((ix >> 2) * H + hx * G + (ix & 3)) * D * 2) + oo;
  unsigned gso = WS_PO + wave * 128u;
  asm volatile("" : "+v"(psel), "+v"(op), "+v"(og[0]), "+v"(og[1]), "+s"(gso));
  __builtin_amdgcn_sched_barrier(0);
  if (!guest && ((need >> sp) & 1u)) __builtin_amdgcn_s_sleep(10);
  while ((int)(now_ns() - tpoll) < 0) __builtin_amdgcn_s_sleep(1);
#pragma unroll
  for (int t = 0; t < 2; ++t)
    if (8 * t + (lane >> 3) < nsrc) og[t] = __builtin_amdgcn_raw_buffer_load_b128(rws, go[t], gso, AUX_SC1);
  float Mr, Wr;
  f32x2 ar;
  auto batch = [&](bool first) {
    auto stale = [&](const u32x4& v) { return (v[1] ^ tg8) | (v[3] ^ tg8); };
    if (__builtin_expect(__builtin_amdgcn_ballot_w64((stale(og[0]) | stale(og[1])) > 0xFFu) != 0ull, 0)) {
      unsigned spins = 0;
      do {
        __builtin_amdgcn_s_sleep(3);
        asm volatile("" ::: "memory");
        if (stale(og[0]) > 0xFFu) og[0] = __builtin_amdgcn_raw_buffer_load_b128(rws, go[0], gso, AUX_SC1);
        if (stale(og[1]) > 0xFFu) og[1] = __builtin_amdgcn_raw_buffer_load_b128(rws, go[1], gso, AUX_SC1);
      } while (__builtin_amdgcn_ballot_w64((stale(og[0]) | stale(og[1])) > 0xFFu) != 0ull && ++spins < SPIN_CAP);
    }
    const unsigned h0 = __builtin_amdgcn_perm(og[0][3], og[0][1], psel), h1 = __builtin_amdgcn_perm(og[1][3], og[1][1], psel);
    const float l0 = __uint_as_float(h0 | dpp<XOR1>(h0)), l1 = __uint_as_float(h1 | dpp<XOR1>(h1));
    const float M = group_max(l0, first ? l1 : fmaxf(l1, Mr));
    const float w0 = __builtin_amdgcn_exp2f(l0 - M), w1 = __builtin_amdgcn_exp2f(l1 - M);
    const float wsum = w0 + w1;
    const f32x2 part = {fmaf(w0, __uint_as_float(og[0][0]), w1 * __uint_as_float(og[1][0])), fmaf(w0, __uint_as_float(og[0][2]), w1 * __uint_as_float(og[1][2]))};
    if (first) {
      Wr = wsum;
      ar = part;
    } else {
      const float wr = __builtin_amdgcn_exp2f(Mr - M);
      Wr = Wr * wr + wsum;
      ar = ar * wr + part;
    }
    Mr = M;
  };
  batch(true);
  for (int k0 = 16; __builtin_expect(k0 < nsrc, 0); k0 += 16) {
    gather_init(k0);
#pragma unroll
    for (int t = 0; t < 2; ++t) go[t] = slot_off(k0 + 8 * t, lane, row_base());
    batch(false);
  }
  const auto sw = __builtin_amdgcn_permlane32_swap(__float_as_uint(ar[0]), __float_as_uint(ar[1]), false, false);
  const auto ww = __builtin_amdgcn_permlane32_swap(__float_as_uint(Wr), __float_as_uint(Wr), false, false);
  const float z0 = __uint_as_float(sw[0]) + __uint_as_float(sw[1]), w2 = __uint_as_float(ww[0]) + __uint_as_float(ww[1]);
  const auto s16 = __builtin_amdgcn_permlane16_swap(__float_as_uint(z0), __float_as_uint(w2), false, false);
  float y = __uint_as_float(s16[0]) + __uint_as_float(s16[1]);
  y += dppf<ROR8>(y);
  const auto f16 = __builtin_amdgcn_permlane16_swap(__float_as_uint(y), __float_as_uint(y), false, false);
  const unsigned pk = pack_bf16(__uint_as_float(f16[0]) * __builtin_amdgcn_rcpf(__uint_as_float(f16[1])), 0.f);
  if (__builtin_amdgcn_inverse_ballot_w64(0x000000FF000000FFull)) *(__attribute__((address_space(1))) unsigned short*)op = (unsigned short)pk;
}

}

extern "C" void kernel_launch(void* const* d_in, const int* in_sizes, int n_in, void* d_out, int out_size, void* d_ws,
                              size_t ws_size, hipStream_t stream) {
  const unsigned cache_bytes = (unsigned)in_sizes[1] * 2u;
  attn_paged<<<NWG, NT, 0, stream>>>(d_in[0], d_in[1], d_in[2], d_in[3], d_in[4], d_out, d_ws, cache_bytes, 0u);
}
```

```cpp
#include <hip/hip_runtime.h>
#include <hip/hip_bf16.h>
#include <cstdint>

namespace {

constexpr int B = 8, H = 32, HKV = 8, G = H / HKV, D = 128, BS = 16, NB = 512, LK = NB * BS;
constexpr int NWG = 256, NW = 8, NT = NW * 64;
constexpr int ROWS = B * G;
constexpr int PAGE_BYTES = BS * HKV * D * 2;
constexpr int TOK_BYTES = HKV * D * 2;
constexpr float SCL = 0.08838834764831845f * 1.4426950408889634f;
constexpr float MINIT = -1e30f;

constexpr int GUESTS = 2;
constexpr int NSF = 32 - GUESTS, NSS = 32 + 3 * GUESTS;
constexpr int SLOTS = 48;
constexpr int MAX_ITEMS = 24;
constexpr int WARM = 5;
constexpr int CODE_LINES = 223;

constexpr float PACE = 67.2f;
constexpr int T0_NS = 1900;
constexpr int B2_NS = 480;
constexpr float EARLY_NS = 290.f, GUEST_NS = 300.f;
constexpr int SPRINT_NS = 170;
constexpr int POLL_NS = 650;
constexpr float FA = 0.23f, FB = 0.36f;
constexpr int TP_NS = 300, TC_NS = 1300;
constexpr float E0_NS = 790.f, GX1 = 5.f, XB = 450.f, GX2 = 2.5f, XMAX = 2300.f;
constexpr int LEAD = 4, TOUCH_AT = 3, REF_A = 14, REF_B = 23;
constexpr unsigned SPIN_CAP = 1u << 20;

constexpr unsigned WS_EPOCH = 0;
constexpr unsigned WS_HDR = 4096;
constexpr unsigned WS_PROBE = 8192;
constexpr unsigned WS_PO = WS_PROBE + 128 * 128;
constexpr unsigned WS_END = WS_PO + HKV * ROWS * SLOTS * 1024;

constexpr int AUX_NT = 2, AUX_SC1 = 16;
constexpr unsigned OOB = 0x80000000u;

using u32x4 = __attribute__((ext_vector_type(4))) unsigned;
using u32x16 = __attribute__((ext_vector_type(16))) unsigned;
using i32x8 = __attribute__((ext_vector_type(8))) int;
using u32x2 = __attribute__((ext_vector_type(2))) unsigned;
using f32x4 = __attribute__((ext_vector_type(4))) float;
using f32x2 = __attribute__((ext_vector_type(2))) float;
using bf16x8 = __attribute__((ext_vector_type(8))) __bf16;
using bf16x2 = __attribute__((ext_vector_type(2))) __bf16;
using s16x4 = __attribute__((ext_vector_type(4))) short;
typedef __attribute__((address_space(1))) unsigned gu32;
typedef __attribute__((address_space(1))) char gchar;
typedef __attribute__((address_space(1))) u32x4 gu32x4;

constexpr int OROW = D + 4, BTW = 68, QROW = 17;
struct Lds {
  union {
    struct {
      u32x4 q[ROWS][QROW];
      int bt[B][NW][BTW];
    } pro;
    float o[NW][ROWS][OROW];
  } u;
  f32x2 ml[NW][ROWS];
  u32x2 items[NW][MAX_ITEMS];
  u32x4 prod[NW];
  u32x2 own[NW];
  unsigned tag;
  unsigned done;
  unsigned ready;
  unsigned lat;
  int ext;
  unsigned claim;
  unsigned pad[2];
};
constexpr int EXT_NONE = -0x40000000;

#define WG_RELAXED __ATOMIC_RELAXED, __HIP_MEMORY_SCOPE_WORKGROUP
#define DEV_RELAXED __ATOMIC_RELAXED, __HIP_MEMORY_SCOPE_AGENT

__device__ __forceinline__ __amdgpu_buffer_rsrc_t make_rsrc(const void* p, unsigned bytes) {
  return __builtin_amdgcn_make_buffer_rsrc((void*)p, 0, bytes, 0x00020000);
}
__device__ __forceinline__ void lds_barrier() { asm volatile("s_waitcnt lgkmcnt(0)\n\ts_barrier" ::: "memory"); }
__device__ __forceinline__ unsigned now_ns() { return (unsigned)__builtin_amdgcn_s_memrealtime() * 10u; }
__device__ __forceinline__ bool loads_pending() { return (__builtin_amdgcn_s_getreg(7 | (3 << 11)) & 15) != 0; }

template <int CTRL>
__device__ __forceinline__ unsigned dpp(unsigned x) {
  return (unsigned)__builtin_amdgcn_mov_dpp((int)x, CTRL, 0xf, 0xf, true);
}
template <int CTRL>
__device__ __forceinline__ float dppf(float x) {
  return __uint_as_float(dpp<CTRL>(__float_as_uint(x)));
}
constexpr int XOR1 = 0xB1, XOR2 = 0x4E, HMIR = 0x141, ROR8 = 0x128;

__device__ __forceinline__ float xrow_max(float x) {
  auto s = __builtin_amdgcn_permlane16_swap(__float_as_uint(x), __float_as_uint(x), false, false);
  x = fmaxf(__uint_as_float(s[0]), __uint_as_float(s[1]));
  auto t = __builtin_amdgcn_permlane32_swap(__float_as_uint(x), __float_as_uint(x), false, false);
  return fmaxf(__uint_as_float(t[0]), __uint_as_float(t[1]));
}
__device__ __forceinline__ float xrow_sum(float x) {
  auto s = __builtin_amdgcn_permlane16_swap(__float_as_uint(x), __float_as_uint(x), false, false);
  x = __uint_as_float(s[0]) + __uint_as_float(s[1]);
  auto t = __builtin_amdgcn_permlane32_swap(__float_as_uint(x), __float_as_uint(x), false, false);
  return __uint_as_float(t[0]) + __uint_as_float(t[1]);
}
__device__ __forceinline__ float group_max(float a, float b) {
  float r, t;
  asm volatile(
      "v_max_f32 %0, %2, %3\n\ts_nop 1\n\t"
      "v_max_f32_dpp %0, %0, %0 row_ror:8 row_mask:0xf bank_mask:0xf\n\t"
      "v_mov_b32 %1, %0\n\ts_nop 1\n\t"
      "v_permlane16_swap_b32 %0, %1\n\t"
      "v_max_f32 %0, %0, %1\n\t"
      "v_mov_b32 %1, %0\n\ts_nop 1\n\t"
      "v_permlane32_swap_b32 %0, %1\n\t"
      "v_max_f32 %0, %0, %1"
      : "=&v"(r), "=&v"(t)
      : "v"(a), "v"(b));
  return r;
}
__device__ __forceinline__ float max8(float x) {
  x = fmaxf(x, dppf<XOR1>(x));
  x = fmaxf(x, dppf<XOR2>(x));
  return fmaxf(x, dppf<HMIR>(x));
}
__device__ __forceinline__ float sum8(float x) {
  x += dppf<XOR1>(x);
  x += dppf<XOR2>(x);
  return x + dppf<HMIR>(x);
}
__device__ __forceinline__ float sum16(float x) {
  x = sum8(x);
  return x + dppf<ROR8>(x);
}
__device__ __forceinline__ unsigned or8(unsigned x) {
  x |= dpp<XOR1>(x);
  x |= dpp<XOR2>(x);
  return x | dpp<HMIR>(x);
}
__device__ __forceinline__ unsigned wave_or(unsigned x) {
  x = or8(x);
  x |= dpp<ROR8>(x);
  auto s = __builtin_amdgcn_permlane16_swap(x, x, false, false);
  x = s[0] | s[1];
  auto t = __builtin_amdgcn_permlane32_swap(x, x, false, false);
  return t[0] | t[1];
}
__device__ __forceinline__ unsigned long long or8_u64(unsigned lo, unsigned hi) {
  return (unsigned)__builtin_amdgcn_readfirstlane(or8(lo)) | ((unsigned long long)(unsigned)__builtin_amdgcn_readfirstlane(or8(hi)) << 32);
}
__device__ __forceinline__ unsigned pack_bf16(float lo, float hi) {
  return __builtin_bit_cast(unsigned, __builtin_convertvector(f32x2{lo, hi}, bf16x2));
}
__device__ __forceinline__ int mbcnt64(unsigned long long m, unsigned z = 0u) {
  return (int)__builtin_amdgcn_mbcnt_hi((unsigned)(m >> 32), __builtin_amdgcn_mbcnt_lo((unsigned)m, z));
}

#define XCHG_BODY(QP)                                                                 \
  "v_cndmask_b32_dpp %0, %12, %8, vcc " QP " row_mask:0xf bank_mask:0xf\n\t"          \
  "v_cndmask_b32_dpp %1, %13, %9, vcc " QP " row_mask:0xf bank_mask:0xf\n\t"          \
  "v_cndmask_b32_dpp %2, %14, %10, vcc " QP " row_mask:0xf bank_mask:0xf\n\t"         \
  "v_cndmask_b32_dpp %3, %15, %11, vcc " QP " row_mask:0xf bank_mask:0xf\n\t"         \
  "s_not_b64 vcc, vcc\n\t"                                                            \
  "v_cndmask_b32_dpp %4, %8, %12, vcc " QP " row_mask:0xf bank_mask:0xf\n\t"          \
  "v_cndmask_b32_dpp %5, %9, %13, vcc " QP " row_mask:0xf bank_mask:0xf\n\t"          \
  "v_cndmask_b32_dpp %6, %10, %14, vcc " QP " row_mask:0xf bank_mask:0xf\n\t"         \
  "v_cndmask_b32_dpp %7, %11, %15, vcc " QP " row_mask:0xf bank_mask:0xf"
#define XCHG_OPS                                                                              \
  : "=&v"(x0), "=&v"(x1), "=&v"(x2), "=&v"(x3), "=&v"(y0), "=&v"(y1), "=&v"(y2), "=&v"(y3)    \
  : "v"(a[0]), "v"(a[1]), "v"(a[2]), "v"(a[3]), "v"(b[0]), "v"(b[1]), "v"(b[2]), "v"(b[3])    \
  : "vcc", "scc"
template <int BIT>
__device__ __forceinline__ void xchg(const u32x4& a, const u32x4& b, u32x4& x, u32x4& y) {
  unsigned x0, x1, x2, x3, y0, y1, y2, y3;
  if constexpr (BIT == 0)
    asm volatile("s_mov_b32 vcc_lo, 0x55555555\n\ts_mov_b32 vcc_hi, 0x55555555\n\t" XCHG_BODY("quad_perm:[1,0,3,2]") XCHG_OPS);
  else
    asm volatile("s_mov_b32 vcc_lo, 0x33333333\n\ts_mov_b32 vcc_hi, 0x33333333\n\t" XCHG_BODY("quad_perm:[2,3,0,1]") "\n\ts_nop 1" XCHG_OPS);
  x = u32x4{x0, x1, x2, x3};
  y = u32x4{y0, y1, y2, y3};
}

__global__ void __launch_bounds__(NT, 2)
attn_paged(const void* q_arg, const void* k_arg, const void* v_arg, const void* bt_arg, const void* ctx_arg, void* out_arg,
           void* ws_arg, unsigned cache_bytes_arg, unsigned zero_arg) {
  __shared__ __attribute__((aligned(16))) Lds lds;
  u32x16 ka;
  const auto kp = __builtin_amdgcn_kernarg_segment_ptr();
  asm volatile("s_load_dwordx16 %0, %1, 0x0" : "=&s"(ka) : "s"(kp));
  const int tid = threadIdx.x, lane = tid & 63;
  const int wave = __builtin_amdgcn_readfirstlane(tid >> 6);
  const int n = lane & 15, g = lane >> 4;
  const int bid = blockIdx.x;
  const int hx = bid & 7, ix = bid >> 3;
  const int col = NW * lane + wave;

  unsigned cpf;
  {
    unsigned long long pc;
    asm volatile("s_getpc_b64 %0" : "=s"(pc));
    cpf = *(const gu32*)((pc & ~63ull) + (tid < CODE_LINES ? tid : 0) * 64u);
  }
  unsigned bto = (unsigned)(wave * NB * 4 + lane * 16);
  unsigned wvo = (unsigned)((lane >> 5) * PAGE_BYTES + ((lane & 31) >> 1) * TOK_BYTES + (lane & 1) * 128) +
                 (unsigned)(ix * WARM) * (unsigned)PAGE_BYTES + (unsigned)(hx * D * 2);
  unsigned tvk = ((unsigned)(ix + 32 * lane) << 21) + (unsigned)bid * 4352u;
  unsigned tvw = WS_PO + (unsigned)((((hx & 4) | (lane & 3)) * ROWS + ix) * SLOTS + SLOTS - 1) * 1024u;
  unsigned tvo = (unsigned)(((ix >> 2) * H + hx * G + (ix & 3)) * D * 2);
  tvk = lane < 2 ? tvk : OOB;
  tvw = lane < 4 ? tvw : OOB;
  tvo = lane < 1 ? tvo : OOB;
  asm volatile("" : "+v"(wvo), "+v"(tvk), "+v"(tvw), "+v"(tvo));
  tvk = tid >= 448 ? wvo : tvk;
  asm volatile("" : "+v"(tvk));
  unsigned xv0 = tid >= 384 ? tvk : tvw, xv1 = tid >= 384 ? tvk : tvo;
  asm volatile("s_waitcnt lgkmcnt(0)" : "+s"(ka), "+v"(bto), "+v"(xv0), "+v"(xv1) : "s"(kp));
  auto arg = [&](int k) { return (unsigned long long)ka[2 * k] | ((unsigned long long)ka[2 * k + 1] << 32); };
  const gchar* const q = (const gchar*)arg(0);
  const char* const kc = (const char*)(const gchar*)arg(1);
  const char* const vc = (const char*)(const gchar*)arg(2);
  const gchar* const bt = (const gchar*)arg(3);
  const char* const out = (const char*)(const gchar*)arg(5);
  gchar* const ws = (gchar*)arg(6);
  const unsigned cache_bytes = ka[14];

  const u32x4 bt0 = *(const gu32x4*)(bt + bto);
  const u32x4 bt1 = *(const gu32x4*)(bt + bto + 1024);
  const i32x8 cv = *(const __attribute__((address_space(4))) i32x8*)arg(4);
  unsigned long long t64 = __builtin_amdgcn_s_memrealtime();
  int cl[B];
#pragma unroll
  for (int b = 0; b < B; ++b) cl[b] = cv[b];
  __builtin_amdgcn_sched_barrier(0);
  asm volatile("s_barrier" ::: "memory");

  unsigned pf[6];
  auto warm = [&](int slot, int first, int pages) {
    const unsigned nrec = wave == NW - 1 ? cache_bytes : 0u, wv = (lane >> 5) < pages ? wvo : OOB;
    pf[2 * slot] = __builtin_amdgcn_raw_buffer_load_b32(make_rsrc(kc, nrec), wv, first * PAGE_BYTES, 0);
    pf[2 * slot + 1] = __builtin_amdgcn_raw_buffer_load_b32(make_rsrc(vc, nrec), wv, first * PAGE_BYTES, 0);
    __builtin_amdgcn_sched_barrier(0);
  };
  {
    const bool aux = wave == NW - 3;
    const unsigned nrec = wave >= NW - 2 ? cache_bytes : 0u;
    pf[0] = __builtin_amdgcn_raw_buffer_load_b32(make_rsrc(aux ? (const char*)ws : kc, aux ? WS_END : nrec), xv0, 0, 0);
    pf[1] = __builtin_amdgcn_raw_buffer_load_b32(make_rsrc(aux ? out : vc, aux ? B * H * D * 2u : nrec), xv1, 0, 0);
    __builtin_amdgcn_sched_barrier(0);
  }
  const int hs = (hx & 4) | ((1 - (int)(ka[2] >> 8)) & 3);
  const bool guest = hx != hs && ix >= NSF;
  int h = guest ? hs : hx;
  const int spg = ((hx - hs - 1) & 3) * GUESTS - NSF, spr = hx == hs ? 3 * GUESTS : 0;
  int sp = ix + (guest ? spg : spr);
  asm volatile("" : "+s"(h), "+s"(sp));
  u32x4 qv = *(const gu32x4*)(q + (unsigned)((wave * H + h * G) * D * 2) + lane * 16u);
  const unsigned epoch = __hip_atomic_load((gu32*)(ws + WS_EPOCH), DEV_RELAXED);
  __builtin_amdgcn_sched_barrier(0);
  unsigned koff[2], voff[2];
#pragma unroll
  for (int t = 0; t < 2; ++t) {
    koff[t] = (unsigned)((4 * (n >> 2) + 2 * t) * TOK_BYTES + g * 64 + (n & 3) * 16);
    voff[t] = (unsigned)((4 * g + 2 * t) * TOK_BYTES + n * 16);
  }
  f32x4 o[2][8];
  float m[2], l[2];
#pragma unroll
  for (int hf = 0; hf < 2; ++hf) {
    m[hf] = MINIT;
    l[hf] = 0.f;
#pragma unroll
    for (int j = 0; j < 8; ++j) o[hf][j] = f32x4{0.f, 0.f, 0.f, 0.f};
    asm volatile("" : "+v"(o[hf][0]), "+v"(o[hf][1]), "+v"(o[hf][2]), "+v"(o[hf][3]), "+v"(o[hf][4]), "+v"(o[hf][5]), "+v"(o[hf][6]), "+v"(o[hf][7]),
                 "+v"(m[hf]), "+v"(l[hf]), "+v"(koff[hf]), "+v"(voff[hf]));
  }
  __builtin_amdgcn_sched_barrier(0);
  asm volatile("" : "+s"(t64) : "s"(cl[0]));
  const unsigned tstart = (unsigned)t64;
  __builtin_amdgcn_sched_barrier(0);
  warm(1, 2, 1);
  asm volatile("" : "+s"(cl[0]), "+s"(cl[1]), "+s"(cl[2]), "+s"(cl[3]), "+s"(cl[4]), "+s"(cl[5]), "+s"(cl[6]), "+s"(cl[7]));

  int cw = cl[0];
#pragma unroll
  for (int b = 1; b < B; ++b) cw = (wave == b) ? cl[b] : cw;
  unsigned long long live[B];
  unsigned lvb = 0;
#pragma unroll
  for (int b = 0; b < B; ++b) {
    cl[b] = (cl[b] <= 0 || cl[b] > LK) ? LK : cl[b];
    live[b] = __builtin_amdgcn_ballot_w64(col * BS < cl[b]);
    lvb |= (col * BS < cl[b]) ? (1u << b) : 0u;
  }
  int ctl[2];
#pragma unroll
  for (int hf = 0; hf < 2; ++hf) {
    int c = cl[4 * hf];
#pragma unroll
    for (int a = 1; a < 4; ++a) c = ((n >> 2) == a) ? cl[4 * hf + a] : c;
    ctl[hf] = c - 4 * g;
  }
  asm volatile("" : "+v"(lvb), "+v"(ctl[0]), "+v"(ctl[1]));
#pragma unroll
  for (int e = 0; e < 4; ++e) {
    lds.u.pro.bt[wave][4 * (lane & 1) + e][lane >> 1] = (int)bt0[e];
    lds.u.pro.bt[wave][4 * (lane & 1) + e][32 + (lane >> 1)] = (int)bt1[e];
  }
  lds_barrier();
  warm(2, 3, 2);

  int pv[B];
#pragma unroll
  for (int b = 0; b < B; ++b) pv[b] = lds.u.pro.bt[b][wave][lane];
  unsigned long long lead[B], heavy[B];
  unsigned eqb = 0;
  {
    unsigned long long eq[B];
#pragma unroll
    for (int b = 0; b < B; ++b) {
      pv[b] = (col * BS < cl[b]) ? pv[b] : (b == 0 ? (int)0x80000000 : pv[b - 1]);
      eq[b] = b == 0 ? 0ull : __builtin_amdgcn_ballot_w64(pv[b] == pv[b - 1]);
      if (b) eqb |= (pv[b] == pv[b - 1]) ? (1u << b) : 0u;
    }
    unsigned long long fol = 0;
#pragma unroll
    for (int b = B - 1; b >= 0; --b) {
      lead[b] = live[b] & ~eq[b];
      heavy[b] = lead[b] & fol;
      fol = eq[b] & (live[b] | fol);
    }
  }
  asm volatile("" : "+v"(eqb));
  auto members = [&](int b) { return (((2u << __builtin_ctz(~(eqb >> (b + 1)))) - 1u) << b) & lvb; };
  int base[B + 1], cnt = 0;
  base[0] = 0;
#pragma unroll
  for (int b = 0; b < B; ++b) {
    const int c = __builtin_popcountll(lead[b]);
    cnt += c;
    base[b + 1] = base[b] + 8 * c + __builtin_popcountll(heavy[b]);
  }
  const int total = base[B];
  const unsigned ta = (unsigned)(total * sp);
  const unsigned mg = h == hs ? 0xFFFFFFFFu / NSS + 1u : 0xFFFFFFFFu / NSF + 1u;
  const int lo = (int)__umulhi(ta, mg) + wave, hi = (int)__umulhi(ta + (unsigned)total, mg) + wave;
  int nmine = 0;
#pragma unroll
  for (int b = 0; b < B; ++b) {
    if (base[b] + 7 < hi && base[b + 1] > lo) {
      const int pos = base[b] + 7 - lo + 8 * mbcnt64(lead[b]) + mbcnt64(heavy[b]);
      const unsigned long long sel = lead[b] & __builtin_amdgcn_ballot_w64((unsigned)pos < (unsigned)(hi - lo));
      if (__builtin_amdgcn_inverse_ballot_w64(sel)) lds.items[wave][nmine + mbcnt64(sel)] = u32x2{(unsigned)pv[b], members(b) | ((unsigned)col << 8)};
      nmine += __builtin_popcountll(sel);
    }
  }
  __builtin_amdgcn_fence(__ATOMIC_RELEASE, "wavefront");
  __builtin_amdgcn_wave_barrier();
  __builtin_amdgcn_fence(__ATOMIC_ACQUIRE, "wavefront");
  u32x2 myit = lds.items[wave][lane < MAX_ITEMS ? lane : 0];

  const unsigned hoff = (unsigned)(h * D * 2);
  u32x4 kr[4], vr[4];
  auto issue = [&](u32x4 (&r)[4], const char* src, const unsigned (&off)[2], int i) {
    const unsigned phys = (unsigned)__builtin_amdgcn_readlane((int)myit[0], i);
    const __amdgpu_buffer_rsrc_t rs = make_rsrc(src, i < nmine ? cache_bytes : 0u);
    const unsigned so = i < nmine ? phys * (unsigned)PAGE_BYTES + hoff : 0u;
#pragma unroll
    for (int t = 0; t < 4; ++t) r[t] = __builtin_amdgcn_raw_buffer_load_b128(rs, off[t >> 1] + TOK_BYTES * (t & 1), so, AUX_NT);
  };
  issue(kr, kc, koff, 0);
  __builtin_amdgcn_sched_barrier(0);

  myit[1] = lane < nmine ? myit[1] : 0u;
  {
    const unsigned um = wave_or(myit[1] & 0xFFu);
    if (lane == 0) lds.own[wave] = u32x2{um, (unsigned)cnt};
  }
  if (cw <= 0) qv = u32x4{0u, 0u, 0u, 0u};
  lds.u.pro.q[wave * G + g][n] = qv;
  if (wave == 0 && lane == 0) {
    lds.tag = epoch + 1u;
    lds.done = 0u;
    lds.ready = 0u;
    lds.lat = 0u;
    lds.ext = EXT_NONE;
    lds.claim = 0u;
  }
  lds_barrier();
  const unsigned tready = now_ns();
  u32x4 qf[2][4];
#pragma unroll
  for (int hf = 0; hf < 2; ++hf)
#pragma unroll
    for (int s = 0; s < 4; ++s) qf[hf][s] = lds.u.pro.q[hf * 16 + n][4 * g + s];
  const u32x2 ownv = lds.own[lane & 7];
  const unsigned uall = __builtin_amdgcn_readfirstlane(or8(ownv[0]));
  const int nseq = __builtin_popcount(uall);
  const unsigned tag = __builtin_amdgcn_readfirstlane(lds.tag), tg8 = tag << 8;
  const __amdgpu_buffer_rsrc_t rws = make_rsrc((const char*)ws, WS_END);
  auto opq = [&](int x) { asm volatile("" : "+s"(x)); return x; };
  auto opv = [&](int x) { asm volatile("" : "+v"(x)); return x; };

  const float span = 0.125f * PACE * sum8((float)ownv[1]);
  const float dur = fmaxf(span - EARLY_NS * (float)(nseq - 1) - (guest ? GUEST_NS : 0.f), 0.f);
  int del = __builtin_amdgcn_readfirstlane((int)(dur * __builtin_amdgcn_rcpf((float)(2 * nmine + (nmine == 0))))) & -(nmine > 0);
  const unsigned tb0 = tstart * 10u + T0_NS, tb1 = tready - B2_NS, tbase = (int)(tb1 - tb0) > 0 ? tb1 : tb0;
  unsigned due = tbase + (unsigned)(((wave - LEAD) * del) >> 3);
  unsigned tpoll = tbase + POLL_NS + (unsigned)__builtin_amdgcn_readfirstlane((int)span);
  const unsigned tma = tbase + (unsigned)__builtin_amdgcn_readfirstlane((int)(span * FA));
  const unsigned tmb = tbase + (unsigned)__builtin_amdgcn_readfirstlane((int)(span * FB));
  const bool refwg = ix == REF_A || ix == REF_B;
  auto posto = [&]() { const int b2 = opq(bid); return WS_PROBE + (unsigned)((b2 & 7) + ((b2 >> 3) == REF_B ? 8 : 0)) * 128u; };
  auto claim = [&](unsigned bit) {
    unsigned old = 0;
    if (lane == 0) old = __hip_atomic_fetch_or(&lds.claim, bit, WG_RELAXED);
    return (__builtin_amdgcn_readfirstlane(old) & bit) == 0u;
  };
  bool settled = false;
  unsigned tlast = 0, topen = 0;
  auto pace = [&](int left) {
    unsigned now = now_ns();
    if (!settled && (int)(now - tma) >= 0) {
      if (topen) {
        if (lane == 0) __hip_atomic_fetch_add(&lds.lat, min(now - topen - 120u, 2500u) + (1u << 20), WG_RELAXED);
        topen = 0;
      }
      if ((int)(now - tmb) < 0) {
        if (refwg) {
          bool pend;
          for (;;) {
            now = now_ns();
            pend = loads_pending();
            if (!pend || (int)(now - (due + del)) >= 0) break;
            __builtin_amdgcn_s_sleep(1);
          }
          if (pend) topen = tlast | 1u;
          else if (lane == 0) __hip_atomic_fetch_add(&lds.lat, min(now - tlast, 2500u) + (1u << 20), WG_RELAXED);
        }
      } else {
        if (refwg && (int)(now - (tmb + TP_NS)) >= 0 && claim(1u)) {
          const int ln = opv(lane);
          const unsigned acc = __hip_atomic_load(&lds.lat, WG_RELAXED);
          __builtin_amdgcn_raw_buffer_store_b128(u32x4{acc, tag, tbase, tag}, rws, (ln >> 3) * 2048 + (ln & 7) * 16, posto(), AUX_SC1);
        } else if ((int)(now - (tmb + TC_NS)) >= 0 && claim(2u)) {
          const int ln = opv(lane);
          const u32x4 v = __builtin_amdgcn_raw_buffer_load_b128(rws, ln < 16 ? ln * 128u : OOB, WS_PROBE + (unsigned)(opq(bid) & 7) * 2048u, AUX_SC1);
          const bool ok = ln < 16 && v[1] == tag && v[3] == tag;
          const float skew = sum16(ok ? (float)(int)(v[2] - tbase) : 0.f) * __builtin_amdgcn_rcpf(sum16(ok ? 1.f : 0.f));
          const float sum = sum16(ok ? (float)(v[0] & 0xFFFFFu) : 0.f), cn = sum16(ok ? (float)(v[0] >> 20) : 0.f);
          const float e = sum * __builtin_amdgcn_rcpf(cn) - E0_NS;
          const float x = fminf(fmaxf(fminf(e * GX1, XB + e * GX2), 0.f), XMAX) * (span * (1.f / 15200.f)) + fminf(fmaxf(skew, -900.f), 900.f);
          if (lane == 0) __hip_atomic_store(&lds.ext, cn > 0.f ? (int)x : 0, WG_RELAXED);
        }
        const int x = __builtin_amdgcn_readfirstlane(__hip_atomic_load(&lds.ext, WG_RELAXED));
        if (x != EXT_NONE) {
          settled = true;
          tpoll += (unsigned)x;
          del += __builtin_amdgcn_readfirstlane((int)((float)x * __builtin_amdgcn_rcpf((float)left)));
        }
      }
    }
    due += (unsigned)(left <= 2 ? del - SPRINT_NS : del);
    while ((int)((now = now_ns()) - due) < 0) __builtin_amdgcn_s_sleep(1);
    tlast = now;
  };

  auto producers = [&]() {
    const int b2 = opq(bid), w2 = opq(wave), tb = b2 >> 5;
    if (w2 == 0 && lane == 0) __builtin_amdgcn_raw_buffer_store_b32(tag, rws, 0, WS_HDR + (unsigned)b2 * 4u, AUX_SC1);
    int idx = 0;
    const unsigned z = (unsigned)opv(0);
#pragma unroll
    for (int a = 0; a < B; ++a) {
      const int ia = base[a] + 7 + 8 * mbcnt64(lead[a], z) + mbcnt64(heavy[a], z);
      idx = __builtin_amdgcn_inverse_ballot_w64(a <= tb ? lead[a] : 0ull) ? ia : idx;
    }
    const int s2 = (int)(((float)(((b2 & 7) == hs ? NSS : NSF) * (idx - w2 + 1) - 1) + 0.5f) * __builtin_amdgcn_rcpf((float)opq(total)));
    const unsigned bit = ((lvb >> tb) & 1u) << (s2 & 31), blo = s2 < 32 ? bit : 0u, bhi = s2 < 32 ? 0u : bit;
    const bool shr = ((eqb >> tb) & 1u) || members(tb) != (1u << tb);
    const u32x4 r4 = {wave_or(blo), wave_or(bhi), wave_or(shr ? blo : 0u), wave_or(shr ? bhi : 0u)};
    if (lane == 0) {
      lds.prod[w2] = r4;
      asm volatile("s_waitcnt lgkmcnt(0)" ::: "memory");
      __hip_atomic_fetch_add(&lds.ready, 1u, WG_RELAXED);
    }
  };
  unsigned long long need = 0;
  int nsrc = 0, srcv = 0, rowv = 0;
  unsigned go[2] = {0u, 0u};
  auto row_base = [&]() { const int b2 = opq(bid); return (unsigned)(((b2 & 7) * ROWS + (b2 >> 3)) * SLOTS); };
  auto slot_off = [&](int k0, int ln, unsigned rb) { return (rb + (unsigned)__builtin_amdgcn_ds_bpermute(4 * (k0 + (ln >> 3)), srcv)) * 1024u + (ln & 7) * 16u; };
  auto resolve = [&]() {
    while (__builtin_amdgcn_readfirstlane(__hip_atomic_load(&lds.ready, WG_RELAXED)) < NW) __builtin_amdgcn_s_sleep(1);
    asm volatile("" ::: "memory");
    const int ln = opv(lane), w2 = opq(wave);
    const unsigned rb = row_base();
    const u32x4 pr = lds.prod[ln & 7];
    need = or8_u64(pr[0], pr[1]);
    const unsigned long long early = or8_u64(pr[2], pr[3]);
    const int seqv = __builtin_amdgcn_ds_permute(ln < B && ((uall >> ln) & 1u) ? 4 * __builtin_popcount(uall & ((1u << (ln & 7)) - 1u)) : 252, ln);
    rowv = 4 * __builtin_amdgcn_ds_bpermute(4 * (ln >> 3), seqv) + (w2 & 3);
    nsrc = __builtin_popcountll(need);
    srcv = __builtin_amdgcn_ds_permute(__builtin_amdgcn_inverse_ballot_w64(need) ? 4 * mbcnt64(need) : 252, ln);
#pragma unroll
    for (int t = 0; t < 2; ++t) go[t] = slot_off(8 * t, ln, rb);
    return __builtin_amdgcn_inverse_ballot_w64(need & ~early) ? WS_PO + (rb + (unsigned)ln) * 1024u + w2 * 128u : WS_END;
  };
  const int ires = min(TOUCH_AT, nmine - 1);

  unsigned tch = 0;
  int i = 0;
  asm volatile("" : "+s"(i));
  do {
    pace(2 * (nmine - i) - 1);
    issue(vr, vc, voff, i);
    __builtin_amdgcn_sched_barrier(0);
    if (i == 0) producers();
    const unsigned meta = (unsigned)__builtin_amdgcn_readlane((int)myit[1], i);
    const int tok0 = (int)(meta >> 8) * BS;
    u32x4 t0, t1, t2, t3, kt[4];
    asm volatile("" ::"v"(tch));
    xchg<0>(kr[0], kr[1], t0, t1);
    xchg<0>(kr[2], kr[3], t2, t3);
    xchg<1>(t0, t2, kt[0], kt[2]);
    xchg<1>(t1, t3, kt[1], kt[3]);
    __builtin_amdgcn_sched_barrier(0);
    unsigned tcho = WS_END;
    if (i == 0 && refwg && wave == NW - 1) {
      const int ln = opv(lane);
      tcho = ln < 8 ? posto() + ln * 2048u : WS_END;
    }
    if (i >= ires && nsrc == 0) tcho = resolve();
    if (i + 1 < nmine) pace(2 * (nmine - i) - 2);
    issue(kr, kc, koff, i + 1);
    tch = __builtin_amdgcn_raw_buffer_load_b32(rws, tcho, 0, AUX_SC1);
    __builtin_amdgcn_sched_barrier(0);
    u32x2 pp[2];
#pragma unroll
    for (int hf = 0; hf < 2; ++hf) {
      if ((meta >> (4 * hf)) & 0xFu) {
        f32x4 s = {0.f, 0.f, 0.f, 0.f};
#pragma unroll
        for (int st = 0; st < 4; ++st)
          s = __builtin_amdgcn_mfma_f32_16x16x32_bf16(__builtin_bit_cast(bf16x8, kt[st]), __builtin_bit_cast(bf16x8, qf[hf][st]), s, 0, 0, 0);
        const int lim = ((meta >> (4 * hf + (n >> 2))) & 1u) ? ctl[hf] - tok0 : 0;
        float x[4], e[4];
#pragma unroll
        for (int r = 0; r < 4; ++r) x[r] = (r < lim) ? s[r] * SCL : -INFINITY;
        const float tm = xrow_max(fmaxf(fmaxf(x[0], x[1]), fmaxf(x[2], x[3])));
        const float mn = fmaxf(m[hf], tm);
        const float al = __builtin_amdgcn_exp2f(m[hf] - mn);
#pragma unroll
        for (int r = 0; r < 4; ++r) e[r] = __builtin_amdgcn_exp2f(x[r] - mn);
        l[hf] = l[hf] * al + ((e[0] + e[1]) + (e[2] + e[3]));
        m[hf] = mn;
#pragma unroll
        for (int j = 0; j < 8; ++j) o[hf][j] *= al;
        pp[hf] = u32x2{pack_bf16(e[0], e[1]), pack_bf16(e[2], e[3])};
      }
    }
    if (i + 1 >= nmine) {
#pragma unroll
      for (int hf = 0; hf < 2; ++hf) {
        const float lt = xrow_sum(l[hf]);
        if (g == 0) lds.ml[opq(wave)][hf * 16 + n] = f32x2{m[hf], lt};
      }
      asm volatile("s_waitcnt lgkmcnt(0)" ::: "memory");
      if (lane == 0) __hip_atomic_fetch_add(&lds.done, 1u, WG_RELAXED);
    }
    __builtin_amdgcn_sched_barrier(0);
    u32x2 vt[8];
#pragma unroll
    for (int w = 0; w < 4; ++w) {
      vt[2 * w] = u32x2{__builtin_amdgcn_perm(vr[1][w], vr[0][w], 0x05040100u), __builtin_amdgcn_perm(vr[3][w], vr[2][w], 0x05040100u)};
      vt[2 * w + 1] = u32x2{__builtin_amdgcn_perm(vr[1][w], vr[0][w], 0x07060302u), __builtin_amdgcn_perm(vr[3][w], vr[2][w], 0x07060302u)};
    }
#pragma unroll
    for (int hf = 0; hf < 2; ++hf) {
      if ((meta >> (4 * hf)) & 0xFu) {
#pragma unroll
        for (int j = 0; j < 8; ++j)
          o[hf][j] = __builtin_amdgcn_mfma_f32_16x16x16bf16_1k(__builtin_bit_cast(s16x4, vt[j]), __builtin_bit_cast(s16x4, pp[hf]), o[hf][j], 0, 0, 0);
      }
    }
    __builtin_amdgcn_sched_barrier(0);
  } while (++i < nmine);

#pragma unroll
  for (int hf = 0; hf < 2; ++hf) {
    const int row = hf * 16 + n;
    if ((uall >> (row >> 2)) & 1u) {
#pragma unroll
      for (int j = 0; j < 8; ++j) *reinterpret_cast<f32x4*>(&lds.u.o[wave][row][16 * j + 4 * g]) = o[hf][j];
    }
  }
  __builtin_amdgcn_s_waitcnt(0x0F70);
  asm volatile("" ::"v"(cpf), "v"(pf[0]), "v"(pf[1]), "v"(pf[2]), "v"(pf[3]), "v"(pf[4]), "v"(pf[5]));

  const int kstep = nseq > 2 ? 2 : 1, kfirst = wave >> 2;
  if (kfirst < kstep) {
    while (__builtin_amdgcn_readfirstlane(__hip_atomic_load(&lds.done, WG_RELAXED)) < NW) __builtin_amdgcn_s_sleep(1);
    asm volatile("" ::: "memory");
    const f32x2 ml = lds.ml[lane & 7][rowv & 31];
    const float M = max8(ml[0]);
    const float wt = __builtin_amdgcn_exp2f(ml[0] - M);
    const float L = sum8(wt * ml[1]);
    const int wn = (int)__float_as_uint(wt * __builtin_amdgcn_rcpf(L));
    const int lsev = (int)__float_as_uint(M + __builtin_amdgcn_logf(L));
    for (int k = kfirst;; k += kstep) {
      const int row = __builtin_amdgcn_readlane(rowv, 8 * k) & 31;
      f32x2 wp[4];
#pragma unroll
      for (int w = 0; w < NW; ++w) wp[w >> 1][w & 1] = __uint_as_float((unsigned)__builtin_amdgcn_readlane(wn, 8 * k + w));
      const unsigned lh = (unsigned)__builtin_amdgcn_readlane(lsev, 8 * k) >> (16 * (lane & 1));
      unsigned tw0 = tg8 | (lh & 0xFFu), tw1 = tg8 | ((lh >> 8) & 0xFFu);
      int e0 = (row * OROW + 2 * lane) * 4, e1 = e0 + 16 * ROWS * OROW;
      unsigned pso = WS_PO + (unsigned)((h * ROWS + row) * SLOTS + sp) * 1024u;
      asm volatile("" : "+v"(tw0), "+v"(tw1), "+v"(e0), "+v"(e1), "+s"(pso), "+v"(wp[0]), "+v"(wp[1]), "+v"(wp[2]), "+v"(wp[3]));
      if (k == kfirst) lds_barrier();
      if (k >= nseq) break;
      const char* img = reinterpret_cast<const char*>(&lds.u.o[0][0][0]);
      f32x2 ov[NW];
#pragma unroll
      for (int w = 0; w < NW; ++w) ov[w] = *reinterpret_cast<const f32x2*>(img + (w < 4 ? e0 : e1) + (w & 3) * (ROWS * OROW * 4));
      f32x2 a0 = wp[0][0] * ov[0], a1 = wp[2][0] * ov[4];
#pragma unroll
      for (int w = 1; w < 4; ++w) {
        a0 += wp[w >> 1][w & 1] * ov[w];
        a1 += wp[2 + (w >> 1)][w & 1] * ov[4 + w];
      }
      a0 += a1;
      __builtin_amdgcn_raw_buffer_store_b128(u32x4{__float_as_uint(a0[0]), tw0, __float_as_uint(a0[1]), tw1}, rws, lane * 16, pso, AUX_SC1);
      if (k + kstep >= nseq) break;
    }
  } else {
    lds_barrier();
  }
  __builtin_amdgcn_sched_barrier(0);

  if (bid == 0 && wave == NW - 2) {
    for (unsigned spins = 0;; ++spins) {
      const u32x4 hd = __builtin_amdgcn_raw_buffer_load_b128(rws, lane * 16, WS_HDR, AUX_SC1);
      const bool ok = hd[0] == tag && hd[1] == tag && hd[2] == tag && hd[3] == tag;
      if (__builtin_amdgcn_ballot_w64(!ok) == 0ull || spins > SPIN_CAP) break;
      __builtin_amdgcn_s_sleep(2);
      asm volatile("" ::: "memory");
    }
    if (lane == 0) __hip_atomic_store((gu32*)(ws + WS_EPOCH), tag, DEV_RELAXED);
  }

  u32x4 og[2];
  auto gather_init = [&](int k0) {
#pragma unroll
    for (int t = 0; t < 2; ++t) {
      const unsigned tw = (k0 + 8 * t + (lane >> 3) < nsrc) ? ~tg8 : tg8;
      og[t] = u32x4{0u, tw | ((lane & 1) ? 0x80u : 0u), 0u, tw | ((lane & 1) ? 0xFFu : 0u)};
    }
  };
  gather_init(0);
  unsigned psel = (lane & 1) ? 0x04000c0cu : 0x0c0c0400u;
  const unsigned oo = (32u * ((lane >> 1) & 3) + 16u * (lane & 1) + 8u * (lane >> 5) + wave) * 2u;
  unsigned long long op = arg(5) + (unsigned)(((ix >> 2) * H + hx * G + (ix & 3)) * D * 2) + oo;
  unsigned gso = WS_PO + wave * 128u;
  asm volatile("" : "+v"(psel), "+v"(op), "+v"(og[0]), "+v"(og[1]), "+s"(gso));
  __builtin_amdgcn_sched_barrier(0);
  if (!guest && ((need >> sp) & 1u)) __builtin_amdgcn_s_sleep(10);
  while ((int)(now_ns() - tpoll) < 0) __builtin_amdgcn_s_sleep(1);
#pragma unroll
  for (int t = 0; t < 2; ++t)
    if (8 * t + (lane >> 3) < nsrc) og[t] = __builtin_amdgcn_raw_buffer_load_b128(rws, go[t], gso, AUX_SC1);
  float Mr, Wr;
  f32x2 ar;
  auto batch = [&](bool first) {
    auto stale = [&](const u32x4& v) { return (v[1] ^ tg8) | (v[3] ^ tg8); };
    if (__builtin_expect(__builtin_amdgcn_ballot_w64((stale(og[0]) | stale(og[1])) > 0xFFu) != 0ull, 0)) {
      unsigned spins = 0;
      do {
        __builtin_amdgcn_s_sleep(3);
        asm volatile("" ::: "memory");
        if (stale(og[0]) > 0xFFu) og[0] = __builtin_amdgcn_raw_buffer_load_b128(rws, go[0], gso, AUX_SC1);
        if (stale(og[1]) > 0xFFu) og[1] = __builtin_amdgcn_raw_buffer_load_b128(rws, go[1], gso, AUX_SC1);
      } while (__builtin_amdgcn_ballot_w64((stale(og[0]) | stale(og[1])) > 0xFFu) != 0ull && ++spins < SPIN_CAP);
    }
    const unsigned h0 = __builtin_amdgcn_perm(og[0][3], og[0][1], psel), h1 = __builtin_amdgcn_perm(og[1][3], og[1][1], psel);
    const float l0 = __uint_as_float(h0 | dpp<XOR1>(h0)), l1 = __uint_as_float(h1 | dpp<XOR1>(h1));
    const float M = group_max(l0, first ? l1 : fmaxf(l1, Mr));
    const float w0 = __builtin_amdgcn_exp2f(l0 - M), w1 = __builtin_amdgcn_exp2f(l1 - M);
    const float wsum = w0 + w1;
    const f32x2 part = {fmaf(w0, __uint_as_float(og[0][0]), w1 * __uint_as_float(og[1][0])), fmaf(w0, __uint_as_float(og[0][2]), w1 * __uint_as_float(og[1][2]))};
    if (first) {
      Wr = wsum;
      ar = part;
    } else {
      const float wr = __builtin_amdgcn_exp2f(Mr - M);
      Wr = Wr * wr + wsum;
      ar = ar * wr + part;
    }
    Mr = M;
  };
  batch(true);
  for (int k0 = 16; __builtin_expect(k0 < nsrc, 0); k0 += 16) {
    gather_init(k0);
#pragma unroll
    for (int t = 0; t < 2; ++t) go[t] = slot_off(k0 + 8 * t, lane, row_base());
    batch(false);
  }
  const auto sw = __builtin_amdgcn_permlane32_swap(__float_as_uint(ar[0]), __float_as_uint(ar[1]), false, false);
  const auto ww = __builtin_amdgcn_permlane32_swap(__float_as_uint(Wr), __float_as_uint(Wr), false, false);
  const float z0 = __uint_as_float(sw[0]) + __uint_as_float(sw[1]), w2 = __uint_as_float(ww[0]) + __uint_as_float(ww[1]);
  const auto s16 = __builtin_amdgcn_permlane16_swap(__float_as_uint(z0), __float_as_uint(w2), false, false);
  float y = __uint_as_float(s16[0]) + __uint_as_float(s16[1]);
  y += dppf<ROR8>(y);
  const auto f16 = __builtin_amdgcn_permlane16_swap(__float_as_uint(y), __float_as_uint(y), false, false);
  const unsigned pk = pack_bf16(__uint_as_float(f16[0]) * __builtin_amdgcn_rcpf(__uint_as_float(f16[1])), 0.f);
  if (__builtin_amdgcn_inverse_ballot_w64(0x000000FF000000FFull)) *(__attribute__((address_space(1))) unsigned short*)op = (unsigned short)pk;
}

}

extern "C" void kernel_launch(void* const* d_in, const int* in_sizes, int n_in, void* d_out, int out_size, void* d_ws,
                              size_t ws_size, hipStream_t stream) {
  const unsigned cache_bytes = (unsigned)in_sizes[1] * 2u;
  attn_paged<<<NWG, NT, 0, stream>>>(d_in[0], d_in[1], d_in[2], d_in[3], d_in[4], d_out, d_ws, cache_bytes, 0u);
}
```
